```python
import torch

def _softmax(x, axis=-1):
    return torch.softmax(x, dim=axis)
def _gelu(x):
    return torch.nn.functional.gelu(x, approximate="tanh")
def _silu(x):
    return torch.nn.functional.silu(x)
def _sigmoid(x):
    return torch.sigmoid(x)
def _layernorm(x, axis=-1, eps=1e-5):
    m = x.mean(dim=axis, keepdim=True)
    v = x.var(dim=axis, keepdim=True, unbiased=False)
    return (x - m) / torch.sqrt(v + eps)
def _scatter_add(tgt, idx, src, axis=0):
    return tgt.clone().index_add_(axis, idx, src)
def _segment_sum(src, seg, n):
    out = torch.zeros((n,) + src.shape[1:], dtype=src.dtype)
    return out.index_add_(0, seg, src)
def _pad(x, pw):
    flat = tuple(v for pair in reversed(pw) for v in pair)
    return torch.nn.functional.pad(x, flat)

kernel_name = 'recurrent_scan__ssm_conv_s4__swiglu_ffn__prompt'

def setup_inputs():

    _w = (lambda a: (lambda t: t.to(torch.bfloat16) if t.is_floating_point() else t)(torch.as_tensor(a)))
    def _A_setup():
        B, L, D, N = 1, 2048, 512, 128
        A_re = -(lambda _x: torch.exp(torch.as_tensor(_x)))((lambda *s: torch.randn(*s, dtype=torch.bfloat16) * 3.2)(D, N))
        A_im = torch.pi * (lambda *s: torch.randn(*s, dtype=torch.bfloat16) * 0.05)(D, N)
        B_re = (lambda *s: torch.randn(*s, dtype=torch.bfloat16) * 0.1)(D, N)
        B_im = (lambda *s: torch.randn(*s, dtype=torch.bfloat16) * 0.1)(D, N)
        C_re = (lambda *s: torch.randn(*s, dtype=torch.bfloat16) * 0.1)(D, N)
        C_im = (lambda *s: torch.randn(*s, dtype=torch.bfloat16) * 0.1)(D, N)
        log_dt = (lambda *s: torch.randn(*s, dtype=torch.bfloat16) * 0.05)(D) * 0.5 - 4.0
        Dskip = (lambda *s: torch.randn(*s, dtype=torch.bfloat16) * 0.05)(D)
        return {
            "u": _w((lambda *s: torch.randn(*s, dtype=torch.bfloat16) * 0.05)(B, L, D)),
            "A_re": _w(A_re),
            "A_im": _w(A_im),
            "B_re": _w(B_re),
            "B_im": _w(B_im),
            "C_re": _w(C_re),
            "C_im": _w(C_im),
            "log_dt": _w(log_dt),
            "Dskip": _w(Dskip),
        }
    _A_in = _A_setup()

    def _B_setup():
        B, S, D, F = 1, 2048, 512, 1792
        return {
            "x": _w((lambda *s: torch.randn(*s, dtype=torch.bfloat16) * 0.05)(B, S, D)),
            "w_gate": _w((lambda *s: torch.randn(*s, dtype=torch.bfloat16) * 0.05)(D, F)),
            "w_up": _w((lambda *s: torch.randn(*s, dtype=torch.bfloat16) * 0.05)(D, F)),
            "w_down": _w((lambda *s: torch.randn(*s, dtype=torch.bfloat16) * 0.05)(F, D)),
        }
    _B_in = _B_setup()

    return {
        "A_u": _A_in["u"],
        "A_A_re": _A_in["A_re"],
        "A_A_im": _A_in["A_im"],
        "A_B_re": _A_in["B_re"],
        "A_B_im": _A_in["B_im"],
        "A_C_re": _A_in["C_re"],
        "A_C_im": _A_in["C_im"],
        "A_log_dt": _A_in["log_dt"],
        "A_Dskip": _A_in["Dskip"],
        "B_x": _B_in["x"],
        "B_w_gate": _B_in["w_gate"],
        "B_w_up": _B_in["w_up"],
        "B_w_down": _B_in["w_down"],
    }

def reference(A_u, A_A_re, A_A_im, A_B_re, A_B_im, A_C_re, A_C_im, A_log_dt, A_Dskip, B_x, B_w_gate, B_w_up, B_w_down):
    def _A_ref(u, A_re, A_im, B_re, B_im, C_re, C_im, log_dt, Dskip):
        Bsz, L, D = u.shape
        dt = torch.exp(log_dt)[:, None]
        A = A_re + 1j * A_im
        Bc = B_re + 1j * B_im
        Cc = C_re + 1j * C_im
        logdA = dt * A
        dB = dt * Bc
        l = (lambda x, dt: x.to(dt))(torch.arange(L), A_re.dtype)
        P = torch.exp(logdA[:, :, None] * l[None, None, :])
        K = torch.einsum("dn,dnl,dn->dl", Cc, P, dB).real
        L2 = 2 * L
        Kp = _pad(K, ((0, 0), (0, L)))
        up = (lambda x, *d: x.permute(*d))(u, 0, 2, 1)
        up = _pad(up, ((0, 0), (0, 0), (0, L)))
        Kf = (lambda x, axes=(-2,-1): torch.fft.rfft2(x.float(), dim=axes))(Kp[:, None, :], axes=(-2, -1))
        Uf = (lambda x, axes=(-2,-1): torch.fft.rfft2(x.float(), dim=axes))(up[:, :, None, :], axes=(-2, -1))
        Yf = Uf * Kf[None, :, :, :]
        y = (lambda x, s, axes=(-2,-1): torch.fft.irfft2(x, s=s, dim=axes).to(torch.bfloat16))(Yf, (1, L2), axes=(-2, -1))[:, :, 0, :L]
        y = (lambda x, *d: x.permute(*d))(y, 0, 2, 1) + u * Dskip[None, None, :]
        return y

    def _B_ref(x, w_gate, w_up, w_down):
        g = _silu(x @ w_gate)
        u = x @ w_up
        return (g * u) @ w_down

    x = A_u
    xn = _layernorm(x, axis=-1)
    s = _A_ref(xn, A_A_re, A_A_im, A_B_re, A_B_im, A_C_re, A_C_im, A_log_dt, A_Dskip)
    g = _silu(_B_ref(xn, B_w_gate, B_w_up, B_w_down))
    return x + s * g
```

```cpp
#include <hip/hip_runtime.h>
#include <stdint.h>

#define GAS __attribute__((address_space(1)))
#define LAS __attribute__((address_space(3)))
typedef __attribute__((ext_vector_type(4))) short s16x4;
typedef __attribute__((ext_vector_type(8))) short s16x8;
typedef __attribute__((ext_vector_type(8))) __bf16 bf16x8;
typedef __attribute__((ext_vector_type(2))) __bf16 bf16x2;
typedef __attribute__((ext_vector_type(2))) float f32x2;
typedef __attribute__((ext_vector_type(4))) float f32x4;
typedef __attribute__((ext_vector_type(16))) float f32x16;
typedef __attribute__((ext_vector_type(4))) unsigned u32x4;
typedef __attribute__((ext_vector_type(2))) unsigned u32x2;
typedef unsigned short u16;

constexpr int SL = 2048, DM = 512, NS = 128, FF = 1792;
constexpr int KR_LEN = 2112;
constexpr int KR_C = 2080;

constexpr size_t WS_XN  = 0;
constexpr size_t WS_XNP = WS_XN + (size_t)SL * DM * 2;
constexpr size_t WS_KR  = WS_XNP + (size_t)SL * DM * 2;
constexpr size_t WS_HF  = WS_KR + (size_t)DM * KR_LEN * 2;
constexpr size_t WS_WDF = WS_HF + (size_t)SL * FF * 2;
constexpr size_t WS_SP  = WS_WDF + (size_t)FF * DM * 2;
constexpr size_t WS_END = WS_SP + (size_t)SL * DM * 2;

__device__ __forceinline__ float bf2f(u16 v) { return __uint_as_float(((unsigned)v) << 16); }
__device__ __forceinline__ float bflo(unsigned v) { return __uint_as_float(v << 16); }
__device__ __forceinline__ float bfhi(unsigned v) { return __uint_as_float(v & 0xffff0000u); }
__device__ __forceinline__ unsigned pk2(float a, float b) {
    f32x2 z = {a, b};
    return __builtin_bit_cast(unsigned, __builtin_convertvector(z, bf16x2));
}
__device__ __forceinline__ float rbf(float a) { return bflo(pk2(a, 0.f) & 0xffffu); }
__device__ __forceinline__ float silu_f(float x) { return x * __builtin_amdgcn_rcpf(1.f + __expf(-x)); }
__device__ __forceinline__ f32x4 mfma16(s16x8 a, s16x8 b, f32x4 c) {
    return __builtin_amdgcn_mfma_f32_16x16x32_bf16(__builtin_bit_cast(bf16x8, a), __builtin_bit_cast(bf16x8, b), c, 0, 0, 0);
}
__device__ __forceinline__ f32x16 mfma32(s16x8 a, s16x8 b, f32x16 c) {
    return __builtin_amdgcn_mfma_f32_32x32x16_bf16(__builtin_bit_cast(bf16x8, a), __builtin_bit_cast(bf16x8, b), c, 0, 0, 0);
}

constexpr int NB_LN = 64, NB_KG = 256, NB_WD = 56;
struct cpx { float re, im; };
__device__ __forceinline__ cpx cmul(cpx a, cpx b) { return {a.re * b.re - a.im * b.im, a.re * b.im + a.im * b.re}; }
__device__ __forceinline__ cpx cexp_p(float are2, float airev, float p) {
    float er = __builtin_amdgcn_exp2f(are2 * p);
    float rv = airev * p;
    rv = rv - __builtin_floorf(rv);
    return {er * __builtin_amdgcn_cosf(rv), er * __builtin_amdgcn_sinf(rv)};
}

__device__ __forceinline__ void ln_role(int blk, const u16* __restrict__ x, u16* __restrict__ xn, unsigned* __restrict__ xnp, char* smem) {
    const int tid = threadIdx.x, lane = tid & 63, wave = tid >> 6;
    unsigned* T = (unsigned*)smem;
    u32x4 v[8];
#pragma unroll
    for (int i = 0; i < 8; ++i) v[i] = *(const u32x4*)(x + (size_t)(blk * 32 + wave * 8 + i) * DM + lane * 8);
#pragma unroll
    for (int i = 0; i < 8; ++i) {
        float f[8];
#pragma unroll
        for (int j = 0; j < 4; ++j) { f[2 * j] = bflo(v[i][j]); f[2 * j + 1] = bfhi(v[i][j]); }
        float s = 0.f;
#pragma unroll
        for (int j = 0; j < 8; ++j) s += f[j];
#pragma unroll
        for (int m = 1; m < 64; m <<= 1) s += __shfl_xor(s, m);
        const float mean = s * (1.f / DM);
        float q = 0.f;
#pragma unroll
        for (int j = 0; j < 8; ++j) { float d = f[j] - mean; q += d * d; }
#pragma unroll
        for (int m = 1; m < 64; m <<= 1) q += __shfl_xor(q, m);
        const float var = rbf(q * (1.f / DM));
        const float den = rbf(__builtin_sqrtf(rbf(var + 1e-5f)));
        const float mb = rbf(mean);
        u32x4 o;
#pragma unroll
        for (int j = 0; j < 4; ++j) {
            float a = rbf(f[2 * j] - mb) / den, b = rbf(f[2 * j + 1] - mb) / den;
            o[j] = pk2(a, b);
        }
        const int r = wave * 8 + i;
        *(u32x4*)(xn + (size_t)(blk * 32 + r) * DM + lane * 8) = o;
#pragma unroll
        for (int j = 0; j < 4; ++j) T[r * 257 + lane * 4 + j] = o[j];
    }
    __syncthreads();
#pragma unroll
    for (int it = 0; it < 8; ++it) {
        const int dp = it * 32 + wave * 8 + (lane >> 3), tq = lane & 7;
        u32x4 o;
#pragma unroll
        for (int j = 0; j < 4; ++j) o[j] = T[(4 * tq + j) * 257 + dp];
        *(u32x4*)(xnp + (size_t)dp * SL + blk * 32 + 4 * tq) = o;
    }
}

__device__ __forceinline__ int lag_idx(int l) {
    if (l < 256) return l;
    const int e = 31 - __builtin_clz(l) - 7;
    const int m = (l + (1 << (e - 1)) - 1 + ((l >> e) & 1)) >> e;
    return 128 * e + m;
}

__device__ __forceinline__ void kg_role(int blk, const u16* __restrict__ Are, const u16* __restrict__ Aim, const u16* __restrict__ Bre,
                                        const u16* __restrict__ Bim, const u16* __restrict__ Cre, const u16* __restrict__ Cim,
                                        const u16* __restrict__ logdt, u16* __restrict__ kr, char* smem) {
    const int tid = threadIdx.x, lane = tid & 63, wave = __builtin_amdgcn_readfirstlane(tid >> 6);
    const int d = blk * 2 + (wave >> 1), n = (wave & 1) * 64 + lane;
    unsigned* tab = (unsigned*)(smem + wave * 16384);
    float* kc = (float*)(smem + 65536);
    const float dt = rbf(expf(bf2f(logdt[d])));
    const float are = dt * bf2f(Are[d * NS + n]), aim = dt * bf2f(Aim[d * NS + n]);
    const cpx dB = {dt * bf2f(Bre[d * NS + n]), dt * bf2f(Bim[d * NS + n])};
    const cpx C = {bf2f(Cre[d * NS + n]), bf2f(Cim[d * NS + n])};
    const cpx w = cmul(C, dB);
    const float are2 = are * 1.44269504088896341f, airev = aim * 0.15915494309189535f;
    auto put = [&](int row, cpx v, float sgn) {
        tab[row * 64 + ((((lane >> 2) ^ (row & 15)) << 2) | (lane & 3))] = pk2(v.re, sgn * v.im);
    };
    const cpx z1 = cexp_p(are2, airev, 1.f), z16 = cexp_p(are2, airev, 16.f), z32 = cexp_p(are2, airev, 32.f),
              z64 = cexp_p(are2, airev, 64.f), z128 = cexp_p(are2, airev, 128.f);
    {
        cpx p = {1.f, 0.f};
        cpx z2, z4, z8;
#pragma unroll
        for (int b = 0; b < 16; ++b) {
            put(b, p, 1.f);
            if (b == 2) z2 = p;
            if (b == 4) z4 = p;
            if (b == 8) z8 = p;
            p = cmul(p, z1);
        }
        p = z16;
#pragma unroll
        for (int i = 0; i < 8; ++i) { put(16 + i, p, 1.f); p = cmul(p, z2); }
        p = z32;
#pragma unroll
        for (int i = 0; i < 8; ++i) { put(24 + i, p, 1.f); p = cmul(p, z4); }
        p = z64;
#pragma unroll
        for (int i = 0; i < 8; ++i) { put(32 + i, p, 1.f); p = cmul(p, z8); }
    }
    const int b = lane & 15, g = lane >> 4;
    int rowB[4];
    rowB[0] = b;
    rowB[1] = b < 8 ? 2 * b : 16 + (b - 8);
    rowB[2] = b < 4 ? 4 * b : (b < 8 ? 16 + (2 * b - 8) : 24 + (b - 8));
    rowB[3] = b < 2 ? 8 * b : (b < 4 ? 16 + (4 * b - 8) : (b < 8 ? 24 + (2 * b - 8) : 32 + (b - 8)));
    auto ldfrag = [&](int row, int ks) -> s16x8 {
        return *(const s16x8*)(tab + row * 64 + (((4 * ks + g) ^ (row & 15)) << 2));
    };
    float* kcw = kc + wave * 648;
    {
        cpx p = w;
#pragma unroll
        for (int a = 0; a < 16; ++a) { put(40 + a, p, -1.f); p = cmul(p, z16); }
        p = cmul(w, cexp_p(are2, airev, 256.f));
#pragma unroll
        for (int a = 0; a < 8; ++a) { put(56 + a, p, -1.f); p = cmul(p, z32); }
        f32x4 acc0 = {0, 0, 0, 0}, acc1 = {0, 0, 0, 0};
#pragma unroll
        for (int ks = 0; ks < 4; ++ks) {
            acc0 = mfma16(ldfrag(40 + b, ks), ldfrag(rowB[0], ks), acc0);
            acc1 = mfma16(ldfrag(48 + b, ks), ldfrag(rowB[1], ks), acc1);
        }
#pragma unroll
        for (int r = 0; r < 4; ++r) {
            kcw[16 * (4 * g + r) + b] = acc0[r];
            if (g >= 2) kcw[256 + 16 * (4 * g + r - 8) + b] = acc1[r];
        }
    }
    {
        cpx p = cmul(w, cexp_p(are2, airev, 512.f));
#pragma unroll
        for (int a = 0; a < 8; ++a) { put(40 + a, p, -1.f); p = cmul(p, z64); }
        p = cmul(w, cexp_p(are2, airev, 1024.f));
#pragma unroll
        for (int a = 0; a < 8; ++a) { put(48 + a, p, -1.f); p = cmul(p, z128); }
        put(56, cmul(w, cexp_p(are2, airev, 2048.f)), -1.f);
        f32x4 acc2 = {0, 0, 0, 0}, acc3 = {0, 0, 0, 0};
#pragma unroll
        for (int ks = 0; ks < 4; ++ks) {
            acc2 = mfma16(ldfrag(40 + b, ks), ldfrag(rowB[2], ks), acc2);
            acc3 = mfma16(ldfrag(48 + b, ks), ldfrag(rowB[3], ks), acc3);
        }
#pragma unroll
        for (int r = 0; r < 4; ++r) {
            if (g < 2) { kcw[384 + 16 * (4 * g + r) + b] = acc2[r]; kcw[512 + 16 * (4 * g + r) + b] = acc3[r]; }
        }
        if (g == 2 && b == 0) kcw[640] = acc3[0];
    }
    __syncthreads();
    for (int c = tid; c < 2 * (KR_LEN / 8); c += 256) {
        const int ch = c / (KR_LEN / 8), y0 = (c % (KR_LEN / 8)) * 8;
        const float* k0 = kc + ch * 2 * 648;
        float f[8];
#pragma unroll
        for (int j = 0; j < 8; ++j) {
            const int l = KR_C - (y0 + j);
            float v = 0.f;
            if (l >= 0 && l < SL) { const int ix = lag_idx(l); v = k0[ix] + k0[648 + ix]; }
            f[j] = v;
        }
        u32x4 o = {pk2(f[0], f[1]), pk2(f[2], f[3]), pk2(f[4], f[5]), pk2(f[6], f[7])};
        *(u32x4*)(kr + (size_t)(blk * 2 + ch) * KR_LEN + y0) = o;
    }
}

__device__ __forceinline__ void wd_role(int kt, const u16* __restrict__ wd, u16* __restrict__ wdf, char* smem) {
    const int tid = threadIdx.x;
    u16* T = (u16*)smem;
#pragma unroll
    for (int i = 0; i < 8; ++i) {
        const int c = i * 256 + tid, r = c >> 6, cc = c & 63;
        *(u32x4*)(T + r * 520 + cc * 8) = *(const u32x4*)(wd + (size_t)(kt * 32 + r) * DM + cc * 8);
    }
    __syncthreads();
#pragma unroll
    for (int i = 0; i < 8; ++i) {
        const int c = i * 256 + tid, dt = c >> 6, l = c & 63, dd = l & 15, gp = l >> 4, uu = gp >> 1, gg = gp & 1;
        u16 e[8];
#pragma unroll
        for (int j = 0; j < 8; ++j) e[j] = T[(16 * uu + 8 * (j >> 2) + 4 * gg + (j & 3)) * 520 + 16 * dt + dd];
        u32x4 o;
#pragma unroll
        for (int j = 0; j < 4; ++j) o[j] = (unsigned)e[2 * j] | ((unsigned)e[2 * j + 1] << 16);
        *(u32x4*)(wdf + (((size_t)dt * 56 + kt) * 64 + l) * 8) = o;
    }
}

struct PrepArgs {
    const u16 *x, *Are, *Aim, *Bre, *Bim, *Cre, *Cim, *logdt, *wd;
    char* ws;
};
constexpr int PREP_LDS = 65536 + 4 * 648 * 4;
__global__ __launch_bounds__(256) void k_prep(PrepArgs a) {
    extern __shared__ __attribute__((aligned(16))) char smem[];
    const int b = blockIdx.x;
    if (b < NB_KG) kg_role(b, a.Are, a.Aim, a.Bre, a.Bim, a.Cre, a.Cim, a.logdt, (u16*)(a.ws + WS_KR), smem);
    else if (b < NB_KG + NB_LN) ln_role(b - NB_KG, a.x, (u16*)(a.ws + WS_XN), (unsigned*)(a.ws + WS_XNP), smem);
    else wd_role(b - NB_KG - NB_LN, a.wd, (u16*)(a.ws + WS_WDF), smem);
}


#define GLDS_ONE(v) "global_load_lds_dwordx4 " v ", %1\n\ts_add_u32 m0, m0, 0x400\n\ts_nop 0\n\t"
__device__ __forceinline__ void glds4(const char* g, unsigned lds, const unsigned (&o)[4]) {
    unsigned keep;
    asm volatile("s_mov_b32 %0, m0\n\ts_mov_b32 m0, %2\n\ts_nop 0\n\t" GLDS_ONE("%3") GLDS_ONE("%4") GLDS_ONE("%5") GLDS_ONE("%6") "s_mov_b32 m0, %0"
                 : "=&s"(keep) : "s"(g), "s"(lds), "v"(o[0]), "v"(o[1]), "v"(o[2]), "v"(o[3]) : "memory", "scc");
}
__device__ __forceinline__ void glds7(const char* g, unsigned lds, const unsigned (&o)[7]) {
    unsigned keep;
    asm volatile("s_mov_b32 %0, m0\n\ts_mov_b32 m0, %2\n\ts_nop 0\n\t" GLDS_ONE("%3") GLDS_ONE("%4") GLDS_ONE("%5") GLDS_ONE("%6")
                 GLDS_ONE("%7") GLDS_ONE("%8") GLDS_ONE("%9") "s_mov_b32 m0, %0"
                 : "=&s"(keep) : "s"(g), "s"(lds), "v"(o[0]), "v"(o[1]), "v"(o[2]), "v"(o[3]), "v"(o[4]), "v"(o[5]), "v"(o[6]) : "memory", "scc");
}

constexpr int G1_XB = 16384;
constexpr int G1_UPOFF = 896 * 16 + 128;
constexpr int G1_WB = G1_UPOFF + 896 * 16;
constexpr int G1_STAGE = G1_XB + G1_WB;
constexpr int G1_LDS = 3 * G1_STAGE;

__global__ __launch_bounds__(256, 1) void k_gemm1(const u16* __restrict__ xn, const u16* __restrict__ wg, const u16* __restrict__ wu, u16* __restrict__ hf) {
    extern __shared__ __attribute__((aligned(16))) char smem[];
    const int tid = threadIdx.x, lane = tid & 63, wave = __builtin_amdgcn_readfirstlane(tid >> 6);
    const int b = blockIdx.x, xcd = b & 7, ix = b >> 3;
    const int rb = (xcd & 1) * 8 + (ix & 7), cb = (xcd >> 1) * 4 + (ix >> 3);
    const int wm = wave & 1, wn = wave >> 1;
    unsigned xoff[4], woff[7];
#pragma unroll
    for (int i = 0; i < 4; ++i) {
        const int ci = (wave * 4 + i) * 64 + lane, row = ci >> 3, pos = ci & 7, ch = pos ^ ((row >> 1) & 7);
        xoff[i] = ((rb * 128 + row) * DM + ch * 8) * 2;
    }
#pragma unroll
    for (int i = 0; i < 7; ++i) {
        const int ci = ((wave & 1) * 7 + i) * 64 + lane, k = ci / 14, c = ci % 14;
        woff[i] = (k * FF + cb * 112 + (c ^ ((k >> 3) & 1)) * 8) * 2;
    }
    const char* xb = (const char*)xn;
    const char* wb = (const char*)(wave >= 2 ? wu : wg);
    const int wdst = G1_XB + (wave >= 2 ? G1_UPOFF : 0) + (wave & 1) * 7 * 1024;
    const unsigned lds0 = (unsigned)(size_t)(LAS char*)smem;
    auto stage = [&](int s, int buf) {
        const unsigned base = lds0 + buf * G1_STAGE;
        glds4(xb + s * 128, base + wave * 4096, xoff);
        glds7(wb + (size_t)s * (64 * FF * 2), base + wdst, woff);
    };
    const int m = lane & 15, g = lane >> 4, q = (lane & 15) >> 2, p = lane & 3;
    const int f = (m >> 1) & 7;
    int xrd[2];
#pragma unroll
    for (int ks = 0; ks < 2; ++ks) xrd[ks] = (wm * 64 + m) * 128 + (((4 * ks + g) ^ f) << 4);
    const int bb = g & 1;
    const int wrd0 = G1_XB + (p >> 1) * G1_UPOFF + 224 * (8 * g + q) + 8 * (p & 1) + wn * 7 * 16;
    const int wrd_e = wrd0 + 16 * bb, wrd_o = wrd0 - 16 * bb;

    f32x4 acc[7][4];
#pragma unroll
    for (int i = 0; i < 7; ++i)
#pragma unroll
        for (int j = 0; j < 4; ++j) acc[i][j] = f32x4{0, 0, 0, 0};

    stage(0, 0);
    stage(1, 1);
#pragma unroll 1
    for (int s = 0; s < 8; ++s) {
        if (s < 7) asm volatile("s_waitcnt vmcnt(11)" ::: "memory"); else asm volatile("s_waitcnt vmcnt(0)" ::: "memory");
        __builtin_amdgcn_s_barrier();
        const int buf = s % 3;
        if (s + 2 < 8) stage(s + 2, (s + 2) % 3);
        const char* base = smem + buf * G1_STAGE;
#pragma unroll
        for (int ks = 0; ks < 2; ++ks) {
            s16x8 xf[4];
#pragma unroll
            for (int mt = 0; mt < 4; ++mt) xf[mt] = *(const s16x8*)(base + xrd[ks] + mt * 2048);
#pragma unroll
            for (int i = 0; i < 7; ++i) {
                const int c8par = (7 * wn + i) & 1;
                const int a0 = (c8par ? wrd_o : wrd_e) + 16 * i + 224 * 32 * ks;
                s16x4 v0 = __builtin_amdgcn_ds_read_tr16_b64_v4i16((LAS s16x4*)(base + a0));
                s16x4 v1 = __builtin_amdgcn_ds_read_tr16_b64_v4i16((LAS s16x4*)(base + a0 + 224 * 4));
                s16x8 wf = __builtin_shufflevector(v0, v1, 0, 1, 2, 3, 4, 5, 6, 7);
#pragma unroll
                for (int mt = 0; mt < 4; ++mt) acc[i][mt] = mfma16(wf, xf[mt], acc[i][mt]);
            }
        }
        asm volatile("s_waitcnt lgkmcnt(0)" ::: "memory");
    }
    unsigned hv[7][2][2];
#pragma unroll
    for (int i = 0; i < 7; ++i)
#pragma unroll
        for (int mp = 0; mp < 2; ++mp) {
            float hh[4];
#pragma unroll
            for (int r = 0; r < 4; ++r) {
                auto sw = __builtin_amdgcn_permlane32_swap(__float_as_uint(acc[i][2 * mp][r]), __float_as_uint(acc[i][2 * mp + 1][r]), false, false);
                const float gt = rbf(__uint_as_float(sw[0])), up = rbf(__uint_as_float(sw[1]));
                hh[r] = rbf(silu_f(gt)) * up;
            }
            hv[i][mp][0] = pk2(hh[0], hh[1]);
            hv[i][mp][1] = pk2(hh[2], hh[3]);
        }
    const int gg = (lane >> 4) & 1;
#pragma unroll
    for (int mp = 0; mp < 2; ++mp) {
        const int mtile = rb * 8 + wm * 4 + 2 * mp + (lane >> 5);
        char* hb = (char*)hf + (size_t)mtile * 56 * 1024;
        auto dst = [&](int c8) -> char* {
            const int G16 = 7 * cb + (c8 >> 1), kt = G16 >> 1, uu = G16 & 1;
            return hb + kt * 1024 + ((2 * uu + gg) * 16 + m) * 16;
        };
        if (wn == 0) {
#pragma unroll
            for (int i = 0; i < 6; i += 2) *(u32x4*)dst(i) = u32x4{hv[i][mp][0], hv[i][mp][1], hv[i + 1][mp][0], hv[i + 1][mp][1]};
            *(u32x2*)dst(6) = u32x2{hv[6][mp][0], hv[6][mp][1]};
        } else {
            *(u32x2*)(dst(7) + 8) = u32x2{hv[0][mp][0], hv[0][mp][1]};
#pragma unroll
            for (int i = 1; i < 7; i += 2) *(u32x4*)dst(7 + i) = u32x4{hv[i][mp][0], hv[i][mp][1], hv[i + 1][mp][0], hv[i + 1][mp][1]};
        }
    }
}

constexpr int CV_KSTR = 4416;
constexpr int CV_KB = 4 * CV_KSTR;
constexpr int CV_UB = 96 * 80;
constexpr int CV_CH = CV_KB + CV_UB;
constexpr int CV_LDS = 2 * CV_CH;
constexpr int CV_QA = 48;

__device__ __forceinline__ void conv_role(int blk, const unsigned* __restrict__ xnp, const u16* __restrict__ kr, const u16* __restrict__ dskip,
                                          unsigned* __restrict__ sp, char* smem) {
    const int tid = threadIdx.x, lane = tid & 63, wave = __builtin_amdgcn_readfirstlane(tid >> 6);
    {
        const unsigned* src = xnp + (size_t)blk * SL + tid * 8;
        const u32x4 a = *(const u32x4*)src, b = *(const u32x4*)(src + 4);
        u32x4 c0, c1;
        c0[0] = __builtin_amdgcn_perm(a[1], a[0], 0x05040100); c1[0] = __builtin_amdgcn_perm(a[1], a[0], 0x07060302);
        c0[1] = __builtin_amdgcn_perm(a[3], a[2], 0x05040100); c1[1] = __builtin_amdgcn_perm(a[3], a[2], 0x07060302);
        c0[2] = __builtin_amdgcn_perm(b[1], b[0], 0x05040100); c1[2] = __builtin_amdgcn_perm(b[1], b[0], 0x07060302);
        c0[3] = __builtin_amdgcn_perm(b[3], b[2], 0x05040100); c1[3] = __builtin_amdgcn_perm(b[3], b[2], 0x07060302);
        const int idx = 1024 + tid * 8;
        const int off = (idx >> 5) * 80 + (idx & 31) * 2;
        *(u32x4*)(smem + CV_KB + off) = c0;
        *(u32x4*)(smem + CV_CH + CV_KB + off) = c1;
        if (tid < 128) {
            const int zi = tid * 8, zo = (zi >> 5) * 80 + (zi & 31) * 2;
            *(u32x4*)(smem + CV_KB + zo) = u32x4{0, 0, 0, 0};
            *(u32x4*)(smem + CV_CH + CV_KB + zo) = u32x4{0, 0, 0, 0};
        }
    }
    for (int c = tid; c < 2 * (KR_LEN / 8); c += 256) {
        const int ch = c / (KR_LEN / 8), bq = c % (KR_LEN / 8);
        const u16* src = kr + (size_t)(blk * 2 + ch) * KR_LEN + bq * 8;
        const u32x4 own = *(const u32x4*)src;
        u32x4 prev = {0, 0, 0, 0};
        if (bq > 0) prev = *(const u32x4*)(src - 8);
        char* dst = smem + ch * CV_CH + bq * 16;
        *(u32x4*)(dst) = own;
        *(u32x4*)(dst + CV_KSTR) = u32x4{__builtin_amdgcn_alignbit(own[0], prev[3], 16), __builtin_amdgcn_alignbit(own[1], own[0], 16),
                                        __builtin_amdgcn_alignbit(own[2], own[1], 16), __builtin_amdgcn_alignbit(own[3], own[2], 16)};
        *(u32x4*)(dst + 2 * CV_KSTR) = u32x4{prev[3], own[0], own[1], own[2]};
        *(u32x4*)(dst + 3 * CV_KSTR) = u32x4{__builtin_amdgcn_alignbit(prev[3], prev[2], 16), __builtin_amdgcn_alignbit(own[0], prev[3], 16),
                                            __builtin_amdgcn_alignbit(own[1], own[0], 16), __builtin_amdgcn_alignbit(own[2], own[1], 16)};
    }
    __syncthreads();
    const int ch = wave >> 1, wb = wave & 1;
    const char* kb = smem + ch * CV_CH;
    const char* ub = kb + CV_KB;
    const int r = lane & 31, h = lane >> 5;
    const int fa = (r & 3) * CV_KSTR + (KR_C - (r & ~3) + 8 * h) * 2;
    const int ua = (32 + r) * 80 + 16 * h;
    f32x16 acc0, acc1;
#pragma unroll
    for (int i = 0; i < 16; ++i) { acc0[i] = 0.f; acc1[i] = 0.f; }
    auto ldF = [&](int q) -> s16x8 {
        const char* p = kb + fa - 32 * q;
        const u32x2 lo = *(const u32x2*)p, hi = *(const u32x2*)(p + 8);
        return __builtin_bit_cast(s16x8, u32x4{lo[0], lo[1], hi[0], hi[1]});
    };
    auto ldB = [&](int o, int q) -> s16x8 {
        const int qp = (q + 2) / 2 - 1, qq = (q + 2) & 1;
        const int off = (32 * o - qp - qq) * 80 + 32 * qq;
        return *(const s16x8*)(ub + ua + off);
    };
    if (wb == 0) {
#pragma unroll 7
        for (int q = -1; q < CV_QA; ++q) {
            const s16x8 F = ldF(q);
            acc0 = mfma32(F, ldB(0, q), acc0);
            acc1 = mfma32(F, ldB(1, q), acc1);
        }
    } else {
#pragma unroll
        for (int q = CV_QA; q <= 64; ++q) {
            const s16x8 F = ldF(q);
            acc0 = mfma32(F, ldB(0, q), acc0);
            acc1 = mfma32(F, ldB(1, q), acc1);
        }
#pragma unroll 8
        for (int q = 65; q <= 128; ++q) {
            const s16x8 F = ldF(q);
            acc1 = mfma32(F, ldB(1, q), acc1);
        }
    }
    __syncthreads();
    {
        float* X = (float*)(smem + ch * CV_CH);
        const f32x16 send = wb ? acc0 : acc1;
#pragma unroll
        for (int i = 0; i < 4; ++i)
            *(f32x4*)(X + wb * 1024 + i * 256 + lane * 4) = f32x4{send[4 * i], send[4 * i + 1], send[4 * i + 2], send[4 * i + 3]};
        __syncthreads();
        f32x16 y = wb ? acc1 : acc0;
#pragma unroll
        for (int i = 0; i < 4; ++i) {
            const f32x4 o = *(const f32x4*)(X + (1 - wb) * 1024 + i * 256 + lane * 4);
#pragma unroll
            for (int j = 0; j < 4; ++j) y[4 * i + j] += o[j];
        }
        const float ds = bf2f(dskip[blk * 2 + ch]);
        u16* S = (u16*)(smem + ch * CV_CH + 8192);
#pragma unroll
        for (int i = 0; i < 4; ++i) {
            const int t = 1024 * wb + 32 * r + 8 * i + 4 * h;
            const int idx = 1024 + t;
            const u32x2 uu = *(const u32x2*)(ub + (idx >> 5) * 80 + (idx & 31) * 2);
            const float s0 = rbf(y[4 * i]) + rbf(bflo(uu[0]) * ds), s1 = rbf(y[4 * i + 1]) + rbf(bfhi(uu[0]) * ds);
            const float s2 = rbf(y[4 * i + 2]) + rbf(bflo(uu[1]) * ds), s3 = rbf(y[4 * i + 3]) + rbf(bfhi(uu[1]) * ds);
            *(u32x2*)(S + t) = u32x2{pk2(s0, s1), pk2(s2, s3)};
        }
    }
    __syncthreads();
    {
        const u16* S0 = (const u16*)(smem + 8192);
        const u16* S1 = (const u16*)(smem + CV_CH + 8192);
        const u32x4 a = *(const u32x4*)(S0 + tid * 8), b = *(const u32x4*)(S1 + tid * 8);
        u32x4 o0, o1;
        o0[0] = __builtin_amdgcn_perm(b[0], a[0], 0x05040100); o0[1] = __builtin_amdgcn_perm(b[0], a[0], 0x07060302);
        o0[2] = __builtin_amdgcn_perm(b[1], a[1], 0x05040100); o0[3] = __builtin_amdgcn_perm(b[1], a[1], 0x07060302);
        o1[0] = __builtin_amdgcn_perm(b[2], a[2], 0x05040100); o1[1] = __builtin_amdgcn_perm(b[2], a[2], 0x07060302);
        o1[2] = __builtin_amdgcn_perm(b[3], a[3], 0x05040100); o1[3] = __builtin_amdgcn_perm(b[3], a[3], 0x07060302);
        unsigned* dst = sp + (size_t)blk * SL + tid * 8;
        *(u32x4*)dst = o0;
        *(u32x4*)(dst + 4) = o1;
    }
}
__global__ __launch_bounds__(256, 2) void k_conv(const unsigned* __restrict__ xnp, const u16* __restrict__ kr, const u16* __restrict__ dskip, unsigned* __restrict__ sp) {
    extern __shared__ __attribute__((aligned(16))) char smem[];
    conv_role(blockIdx.x, xnp, kr, dskip, sp, smem);
}

constexpr int G2_LDS = 4 * 12 * 1024;
__global__ __launch_bounds__(256, 2) void k_gemm2(const u16* __restrict__ hf, const u16* __restrict__ wdf, const unsigned* __restrict__ sp,
                                                  const u16* __restrict__ x, u16* __restrict__ out) {
    extern __shared__ __attribute__((aligned(16))) char smem[];
    const int tid = threadIdx.x, lane = tid & 63, wave = __builtin_amdgcn_readfirstlane(tid >> 6);
    const int b = blockIdx.x, xcd = b & 7, ix = b >> 3;
    const int rb = xcd * 4 + (ix & 3), cb = ix >> 2;
    const char* hb = (const char*)hf + ((size_t)(rb * 4) * 56 + wave * 14) * 1024 + lane * 16;
    const char* wb = (const char*)wdf + ((size_t)(cb * 4) * 56 + wave * 14) * 1024 + lane * 16;
    f32x4 acc[4][4];
#pragma unroll
    for (int i = 0; i < 4; ++i)
#pragma unroll
        for (int j = 0; j < 4; ++j) acc[i][j] = f32x4{0, 0, 0, 0};
    constexpr int PD = 3;
    s16x8 A[PD][4], B[PD][4];
    auto ld = [&](int kt, int sl) {
#pragma unroll
        for (int i = 0; i < 4; ++i) {
            A[sl][i] = *(const s16x8*)(wb + ((size_t)i * 56 + kt) * 1024);
            B[sl][i] = *(const s16x8*)(hb + ((size_t)i * 56 + kt) * 1024);
        }
    };
#pragma unroll
    for (int i = 0; i < PD - 1; ++i) ld(i, i);
#pragma unroll
    for (int kt = 0; kt < 14; ++kt) {
        if (kt + PD - 1 < 14) ld(kt + PD - 1, (kt + PD - 1) % PD);
#pragma unroll
        for (int i = 0; i < 4; ++i)
#pragma unroll
            for (int j = 0; j < 4; ++j) acc[i][j] = mfma16(A[kt % PD][i], B[kt % PD][j], acc[i][j]);
    }
    f32x4* X = (f32x4*)smem;
#pragma unroll
    for (int j = 0; j < 4; ++j) {
        if (j != wave) {
            const int sl = wave - (wave > j ? 1 : 0);
#pragma unroll
            for (int i = 0; i < 4; ++i) X[((j * 3 + sl) * 4 + i) * 64 + lane] = acc[i][j];
        }
    }
    __syncthreads();
    f32x4 y[4];
#pragma unroll
    for (int i = 0; i < 4; ++i) {
        f32x4 own = wave == 0 ? acc[i][0] : (wave == 1 ? acc[i][1] : (wave == 2 ? acc[i][2] : acc[i][3]));
        f32x4 p[3];
#pragma unroll
        for (int s = 0; s < 3; ++s) p[s] = X[((wave * 3 + s) * 4 + i) * 64 + lane];
        y[i] = own + p[0] + p[1] + p[2];
    }
    const int m = lane & 15, g = lane >> 4;
    const int t = rb * 64 + wave * 16 + m;
#pragma unroll
    for (int i = 0; i < 4; ++i) {
        const int d = cb * 64 + 16 * i + 4 * g;
        const u32x2 xv = *(const u32x2*)(x + (size_t)t * DM + d);
        const unsigned s01 = sp[(size_t)(d >> 1) * SL + t], s23 = sp[(size_t)((d >> 1) + 1) * SL + t];
        const float o0 = bflo(xv[0]) + rbf(bflo(s01) * rbf(silu_f(rbf(y[i][0]))));
        const float o1 = bfhi(xv[0]) + rbf(bfhi(s01) * rbf(silu_f(rbf(y[i][1]))));
        const float o2 = bflo(xv[1]) + rbf(bflo(s23) * rbf(silu_f(rbf(y[i][2]))));
        const float o3 = bfhi(xv[1]) + rbf(bfhi(s23) * rbf(silu_f(rbf(y[i][3]))));
        *(u32x2*)(out + (size_t)t * DM + d) = u32x2{pk2(o0, o1), pk2(o2, o3)};
    }
}

extern "C" void kernel_launch(void* const* d_in, const int* in_sizes, int n_in, void* d_out, int out_size, void* d_ws, size_t ws_size,
                              hipStream_t stream) {
    char* ws = (char*)d_ws;
    PrepArgs pa;
    pa.x = (const u16*)d_in[0]; pa.Are = (const u16*)d_in[1]; pa.Aim = (const u16*)d_in[2]; pa.Bre = (const u16*)d_in[3];
    pa.Bim = (const u16*)d_in[4]; pa.Cre = (const u16*)d_in[5]; pa.Cim = (const u16*)d_in[6]; pa.logdt = (const u16*)d_in[7];
    pa.wd = (const u16*)d_in[12]; pa.ws = ws;
    (void)hipFuncSetAttribute((const void*)k_prep, hipFuncAttributeMaxDynamicSharedMemorySize, PREP_LDS);
    (void)hipFuncSetAttribute((const void*)k_gemm1, hipFuncAttributeMaxDynamicSharedMemorySize, G1_LDS);
    k_prep<<<NB_KG + NB_LN + NB_WD, 256, PREP_LDS, stream>>>(pa);
    k_gemm1<<<256, 256, G1_LDS, stream>>>((const u16*)(ws + WS_XN), (const u16*)d_in[10], (const u16*)d_in[11], (u16*)(ws + WS_HF));
    k_conv<<<256, 256, CV_LDS, stream>>>((const unsigned*)(ws + WS_XNP), (const u16*)(ws + WS_KR), (const u16*)d_in[8], (unsigned*)(ws + WS_SP));
    k_gemm2<<<256, 256, G2_LDS, stream>>>((const u16*)(ws + WS_HF), (const u16*)(ws + WS_WDF), (const unsigned*)(ws + WS_SP),
                                          (const u16*)d_in[0], (u16*)d_out);
}
```

```cpp
#include <hip/hip_runtime.h>
#include <stdint.h>

#define GAS __attribute__((address_space(1)))
#define LAS __attribute__((address_space(3)))
typedef __attribute__((ext_vector_type(4))) short s16x4;
typedef __attribute__((ext_vector_type(8))) short s16x8;
typedef __attribute__((ext_vector_type(8))) __bf16 bf16x8;
typedef __attribute__((ext_vector_type(2))) __bf16 bf16x2;
typedef __attribute__((ext_vector_type(2))) float f32x2;
typedef __attribute__((ext_vector_type(4))) float f32x4;
typedef __attribute__((ext_vector_type(16))) float f32x16;
typedef __attribute__((ext_vector_type(4))) unsigned u32x4;
typedef __attribute__((ext_vector_type(2))) unsigned u32x2;
typedef unsigned short u16;

constexpr int SL = 2048, DM = 512, NS = 128, FF = 1792;
constexpr int KR_LEN = 2112;
constexpr int KR_C = 2080;

constexpr size_t WS_XN  = 0;
constexpr size_t WS_XNP = WS_XN + (size_t)SL * DM * 2;
constexpr size_t WS_KR  = WS_XNP + (size_t)SL * DM * 2;
constexpr size_t WS_HF  = WS_KR + (size_t)DM * KR_LEN * 2;
constexpr size_t WS_WDF = WS_HF + (size_t)SL * FF * 2;
constexpr size_t WS_SP  = WS_WDF + (size_t)FF * DM * 2;
constexpr size_t WS_END = WS_SP + (size_t)SL * DM * 2;

#define ST_DECL
#define ST(i)
#define ST_CLK(i)
#define ST_FLUSH(wsbase, kid)

__device__ __forceinline__ float bf2f(u16 v) { return __uint_as_float(((unsigned)v) << 16); }
__device__ __forceinline__ float bflo(unsigned v) { return __uint_as_float(v << 16); }
__device__ __forceinline__ float bfhi(unsigned v) { return __uint_as_float(v & 0xffff0000u); }
__device__ __forceinline__ unsigned pk2(float a, float b) {
    f32x2 z = {a, b};
    return __builtin_bit_cast(unsigned, __builtin_convertvector(z, bf16x2));
}
__device__ __forceinline__ float rbf(float a) { return bflo(pk2(a, 0.f) & 0xffffu); }
__device__ __forceinline__ float silu_f(float x) { return x * __builtin_amdgcn_rcpf(1.f + __expf(-x)); }
__device__ __forceinline__ f32x4 mfma16(s16x8 a, s16x8 b, f32x4 c) {
    return __builtin_amdgcn_mfma_f32_16x16x32_bf16(__builtin_bit_cast(bf16x8, a), __builtin_bit_cast(bf16x8, b), c, 0, 0, 0);
}
__device__ __forceinline__ f32x16 mfma32(s16x8 a, s16x8 b, f32x16 c) {
    return __builtin_amdgcn_mfma_f32_32x32x16_bf16(__builtin_bit_cast(bf16x8, a), __builtin_bit_cast(bf16x8, b), c, 0, 0, 0);
}

constexpr int NB_LN = 64, NB_KG = 256, NB_WD = 56;
struct cpx { float re, im; };
__device__ __forceinline__ cpx cmul(cpx a, cpx b) { return {a.re * b.re - a.im * b.im, a.re * b.im + a.im * b.re}; }
__device__ __forceinline__ cpx cexp_p(float are2, float airev, float p) {
    float er = __builtin_amdgcn_exp2f(are2 * p);
    float rv = airev * p;
    rv = rv - __builtin_floorf(rv);
    return {er * __builtin_amdgcn_cosf(rv), er * __builtin_amdgcn_sinf(rv)};
}

__device__ __forceinline__ void ln_role(int blk, const u16* __restrict__ x, u16* __restrict__ xn, unsigned* __restrict__ xnp, char* smem) {
    const int tid = threadIdx.x, lane = tid & 63, wave = tid >> 6;
    unsigned* T = (unsigned*)smem;
    u32x4 v[8];
#pragma unroll
    for (int i = 0; i < 8; ++i) v[i] = *(const u32x4*)(x + (size_t)(blk * 32 + wave * 8 + i) * DM + lane * 8);
#pragma unroll
    for (int i = 0; i < 8; ++i) {
        float f[8];
#pragma unroll
        for (int j = 0; j < 4; ++j) { f[2 * j] = bflo(v[i][j]); f[2 * j + 1] = bfhi(v[i][j]); }
        float s = 0.f;
#pragma unroll
        for (int j = 0; j < 8; ++j) s += f[j];
#pragma unroll
        for (int m = 1; m < 64; m <<= 1) s += __shfl_xor(s, m);
        const float mean = s * (1.f / DM);
        float q = 0.f;
#pragma unroll
        for (int j = 0; j < 8; ++j) { float d = f[j] - mean; q += d * d; }
#pragma unroll
        for (int m = 1; m < 64; m <<= 1) q += __shfl_xor(q, m);
        const float var = rbf(q * (1.f / DM));
        const float den = rbf(__builtin_sqrtf(rbf(var + 1e-5f)));
        const float mb = rbf(mean);
        u32x4 o;
#pragma unroll
        for (int j = 0; j < 4; ++j) {
            float a = rbf(f[2 * j] - mb) / den, b = rbf(f[2 * j + 1] - mb) / den;
            o[j] = pk2(a, b);
        }
        const int r = wave * 8 + i;
        *(u32x4*)(xn + (size_t)(blk * 32 + r) * DM + lane * 8) = o;
#pragma unroll
        for (int j = 0; j < 4; ++j) T[r * 257 + lane * 4 + j] = o[j];
    }
    __syncthreads();
#pragma unroll
    for (int it = 0; it < 8; ++it) {
        const int dp = it * 32 + wave * 8 + (lane >> 3), tq = lane & 7;
        u32x4 o;
#pragma unroll
        for (int j = 0; j < 4; ++j) o[j] = T[(4 * tq + j) * 257 + dp];
        *(u32x4*)(xnp + (size_t)dp * SL + blk * 32 + 4 * tq) = o;
    }
}

__device__ __forceinline__ int lag_idx(int l) {
    if (l < 256) return l;
    const int e = 31 - __builtin_clz(l) - 7;
    const int m = (l + (1 << (e - 1)) - 1 + ((l >> e) & 1)) >> e;
    return 128 * e + m;
}

__device__ __forceinline__ void kg_role(int blk, const u16* __restrict__ Are, const u16* __restrict__ Aim, const u16* __restrict__ Bre,
                                        const u16* __restrict__ Bim, const u16* __restrict__ Cre, const u16* __restrict__ Cim,
                                        const u16* __restrict__ logdt, u16* __restrict__ kr, char* smem) {
    const int tid = threadIdx.x, lane = tid & 63, wave = __builtin_amdgcn_readfirstlane(tid >> 6);
    const int d = blk * 2 + (wave >> 1), n = (wave & 1) * 64 + lane;
    unsigned* tab = (unsigned*)(smem + wave * 16384);
    float* kc = (float*)(smem + 65536);
    const float dt = rbf(expf(bf2f(logdt[d])));
    const float are = dt * bf2f(Are[d * NS + n]), aim = dt * bf2f(Aim[d * NS + n]);
    const cpx dB = {dt * bf2f(Bre[d * NS + n]), dt * bf2f(Bim[d * NS + n])};
    const cpx C = {bf2f(Cre[d * NS + n]), bf2f(Cim[d * NS + n])};
    const cpx w = cmul(C, dB);
    const float are2 = are * 1.44269504088896341f, airev = aim * 0.15915494309189535f;
    auto put = [&](int row, cpx v, float sgn) {
        tab[row * 64 + ((((lane >> 2) ^ (row & 15)) << 2) | (lane & 3))] = pk2(v.re, sgn * v.im);
    };
    const cpx z1 = cexp_p(are2, airev, 1.f), z16 = cexp_p(are2, airev, 16.f), z32 = cexp_p(are2, airev, 32.f),
              z64 = cexp_p(are2, airev, 64.f), z128 = cexp_p(are2, airev, 128.f);
    {
        cpx p = {1.f, 0.f};
        cpx z2, z4, z8;
#pragma unroll
        for (int b = 0; b < 16; ++b) {
            put(b, p, 1.f);
            if (b == 2) z2 = p;
            if (b == 4) z4 = p;
            if (b == 8) z8 = p;
            p = cmul(p, z1);
        }
        p = z16;
#pragma unroll
        for (int i = 0; i < 8; ++i) { put(16 + i, p, 1.f); p = cmul(p, z2); }
        p = z32;
#pragma unroll
        for (int i = 0; i < 8; ++i) { put(24 + i, p, 1.f); p = cmul(p, z4); }
        p = z64;
#pragma unroll
        for (int i = 0; i < 8; ++i) { put(32 + i, p, 1.f); p = cmul(p, z8); }
    }
    const int b = lane & 15, g = lane >> 4;
    int rowB[4];
    rowB[0] = b;
    rowB[1] = b < 8 ? 2 * b : 16 + (b - 8);
    rowB[2] = b < 4 ? 4 * b : (b < 8 ? 16 + (2 * b - 8) : 24 + (b - 8));
    rowB[3] = b < 2 ? 8 * b : (b < 4 ? 16 + (4 * b - 8) : (b < 8 ? 24 + (2 * b - 8) : 32 + (b - 8)));
    auto ldfrag = [&](int row, int ks) -> s16x8 {
        return *(const s16x8*)(tab + row * 64 + (((4 * ks + g) ^ (row & 15)) << 2));
    };
    float* kcw = kc + wave * 648;
    {
        cpx p = w;
#pragma unroll
        for (int a = 0; a < 16; ++a) { put(40 + a, p, -1.f); p = cmul(p, z16); }
        p = cmul(w, cexp_p(are2, airev, 256.f));
#pragma unroll
        for (int a = 0; a < 8; ++a) { put(56 + a, p, -1.f); p = cmul(p, z32); }
        f32x4 acc0 = {0, 0, 0, 0}, acc1 = {0, 0, 0, 0};
#pragma unroll
        for (int ks = 0; ks < 4; ++ks) {
            acc0 = mfma16(ldfrag(40 + b, ks), ldfrag(rowB[0], ks), acc0);
            acc1 = mfma16(ldfrag(48 + b, ks), ldfrag(rowB[1], ks), acc1);
        }
#pragma unroll
        for (int r = 0; r < 4; ++r) {
            kcw[16 * (4 * g + r) + b] = acc0[r];
            if (g >= 2) kcw[256 + 16 * (4 * g + r - 8) + b] = acc1[r];
        }
    }
    {
        cpx p = cmul(w, cexp_p(are2, airev, 512.f));
#pragma unroll
        for (int a = 0; a < 8; ++a) { put(40 + a, p, -1.f); p = cmul(p, z64); }
        p = cmul(w, cexp_p(are2, airev, 1024.f));
#pragma unroll
        for (int a = 0; a < 8; ++a) { put(48 + a, p, -1.f); p = cmul(p, z128); }
        put(56, cmul(w, cexp_p(are2, airev, 2048.f)), -1.f);
        f32x4 acc2 = {0, 0, 0, 0}, acc3 = {0, 0, 0, 0};
#pragma unroll
        for (int ks = 0; ks < 4; ++ks) {
            acc2 = mfma16(ldfrag(40 + b, ks), ldfrag(rowB[2], ks), acc2);
            acc3 = mfma16(ldfrag(48 + b, ks), ldfrag(rowB[3], ks), acc3);
        }
#pragma unroll
        for (int r = 0; r < 4; ++r) {
            if (g < 2) { kcw[384 + 16 * (4 * g + r) + b] = acc2[r]; kcw[512 + 16 * (4 * g + r) + b] = acc3[r]; }
        }
        if (g == 2 && b == 0) kcw[640] = acc3[0];
    }
    __syncthreads();
    for (int c = tid; c < 2 * (KR_LEN / 8); c += 256) {
        const int ch = c / (KR_LEN / 8), y0 = (c % (KR_LEN / 8)) * 8;
        const float* k0 = kc + ch * 2 * 648;
        float f[8];
#pragma unroll
        for (int j = 0; j < 8; ++j) {
            const int l = KR_C - (y0 + j);
            float v = 0.f;
            if (l >= 0 && l < SL) { const int ix = lag_idx(l); v = k0[ix] + k0[648 + ix]; }
            f[j] = v;
        }
        u32x4 o = {pk2(f[0], f[1]), pk2(f[2], f[3]), pk2(f[4], f[5]), pk2(f[6], f[7])};
        *(u32x4*)(kr + (size_t)(blk * 2 + ch) * KR_LEN + y0) = o;
    }
}

__device__ __forceinline__ void wd_role(int kt, const u16* __restrict__ wd, u16* __restrict__ wdf, char* smem) {
    const int tid = threadIdx.x;
    u16* T = (u16*)smem;
#pragma unroll
    for (int i = 0; i < 8; ++i) {
        const int c = i * 256 + tid, r = c >> 6, cc = c & 63;
        *(u32x4*)(T + r * 520 + cc * 8) = *(const u32x4*)(wd + (size_t)(kt * 32 + r) * DM + cc * 8);
    }
    __syncthreads();
#pragma unroll
    for (int i = 0; i < 8; ++i) {
        const int c = i * 256 + tid, dt = c >> 6, l = c & 63, dd = l & 15, gp = l >> 4, uu = gp >> 1, gg = gp & 1;
        u16 e[8];
#pragma unroll
        for (int j = 0; j < 8; ++j) e[j] = T[(16 * uu + 8 * (j >> 2) + 4 * gg + (j & 3)) * 520 + 16 * dt + dd];
        u32x4 o;
#pragma unroll
        for (int j = 0; j < 4; ++j) o[j] = (unsigned)e[2 * j] | ((unsigned)e[2 * j + 1] << 16);
        *(u32x4*)(wdf + (((size_t)dt * 56 + kt) * 64 + l) * 8) = o;
    }
}

struct PrepArgs {
    const u16 *x, *Are, *Aim, *Bre, *Bim, *Cre, *Cim, *logdt, *wd;
    char* ws;
};
constexpr int PREP_LDS = 65536 + 4 * 648 * 4;
__global__ __launch_bounds__(256) void k_prep(PrepArgs a) {
    extern __shared__ __attribute__((aligned(16))) char smem[];
    const int b = blockIdx.x;
    ST_DECL; ST(0); ST_CLK(6);
    if (b < NB_KG) kg_role(b, a.Are, a.Aim, a.Bre, a.Bim, a.Cre, a.Cim, a.logdt, (u16*)(a.ws + WS_KR), smem);
    else if (b < NB_KG + NB_LN) ln_role(b - NB_KG, a.x, (u16*)(a.ws + WS_XN), (unsigned*)(a.ws + WS_XNP), smem);
    else wd_role(b - NB_KG - NB_LN, a.wd, (u16*)(a.ws + WS_WDF), smem);
    ST(5); ST_CLK(7); ST_FLUSH(a.ws, 0);
}

#define GLDS_ONE(v) "global_load_lds_dwordx4 " v ", %1\n\ts_add_u32 m0, m0, 0x400\n\ts_nop 0\n\t"
__device__ __forceinline__ void glds4(const char* g, unsigned lds, const unsigned (&o)[4]) {
    unsigned keep;
    asm volatile("s_mov_b32 %0, m0\n\ts_mov_b32 m0, %2\n\ts_nop 0\n\t" GLDS_ONE("%3") GLDS_ONE("%4") GLDS_ONE("%5") GLDS_ONE("%6") "s_mov_b32 m0, %0"
                 : "=&s"(keep) : "s"(g), "s"(lds), "v"(o[0]), "v"(o[1]), "v"(o[2]), "v"(o[3]) : "memory", "scc");
}
__device__ __forceinline__ void glds7(const char* g, unsigned lds, const unsigned (&o)[7]) {
    unsigned keep;
    asm volatile("s_mov_b32 %0, m0\n\ts_mov_b32 m0, %2\n\ts_nop 0\n\t" GLDS_ONE("%3") GLDS_ONE("%4") GLDS_ONE("%5") GLDS_ONE("%6")
                 GLDS_ONE("%7") GLDS_ONE("%8") GLDS_ONE("%9") "s_mov_b32 m0, %0"
                 : "=&s"(keep) : "s"(g), "s"(lds), "v"(o[0]), "v"(o[1]), "v"(o[2]), "v"(o[3]), "v"(o[4]), "v"(o[5]), "v"(o[6]) : "memory", "scc");
}

constexpr int G1_XB = 16384;
constexpr int G1_UPOFF = 896 * 16 + 128;
constexpr int G1_WB = G1_UPOFF + 896 * 16;
constexpr int G1_STAGE = G1_XB + G1_WB;
constexpr int G1_LDS = 3 * G1_STAGE;

__global__ __launch_bounds__(256, 1) void k_gemm1(const u16* __restrict__ xn, const u16* __restrict__ wg, const u16* __restrict__ wu, u16* __restrict__ hf) {
    extern __shared__ __attribute__((aligned(16))) char smem[];
    ST_DECL; ST(0); ST_CLK(6);
    const int tid = threadIdx.x, lane = tid & 63, wave = __builtin_amdgcn_readfirstlane(tid >> 6);
    const int b = blockIdx.x, xcd = b & 7, ix = b >> 3;
    const int rb = (xcd & 1) * 8 + (ix & 7), cb = (xcd >> 1) * 4 + (ix >> 3);
    const int wm = wave & 1, wn = wave >> 1;
    unsigned xoff[4], woff[7];
#pragma unroll
    for (int i = 0; i < 4; ++i) {
        const int ci = (wave * 4 + i) * 64 + lane, row = ci >> 3, pos = ci & 7, ch = pos ^ ((row >> 1) & 7);
        xoff[i] = ((rb * 128 + row) * DM + ch * 8) * 2;
    }
#pragma unroll
    for (int i = 0; i < 7; ++i) {
        const int ci = ((wave & 1) * 7 + i) * 64 + lane, k = ci / 14, c = ci % 14;
        woff[i] = (k * FF + cb * 112 + (c ^ ((k >> 3) & 1)) * 8) * 2;
    }
    const char* xb = (const char*)xn;
    const char* wb = (const char*)(wave >= 2 ? wu : wg);
    const int wdst = G1_XB + (wave >= 2 ? G1_UPOFF : 0) + (wave & 1) * 7 * 1024;
    const unsigned lds0 = (unsigned)(size_t)(LAS char*)smem;
    auto stage = [&](int s, int buf) {
        const unsigned base = lds0 + buf * G1_STAGE;
        glds4(xb + s * 128, base + wave * 4096, xoff);
        glds7(wb + (size_t)s * (64 * FF * 2), base + wdst, woff);
    };
    const int m = lane & 15, g = lane >> 4, q = (lane & 15) >> 2, p = lane & 3;
    const int f = (m >> 1) & 7;
    int xrd[2];
#pragma unroll
    for (int ks = 0; ks < 2; ++ks) xrd[ks] = (wm * 64 + m) * 128 + (((4 * ks + g) ^ f) << 4);
    const int bb = g & 1;
    const int wrd0 = G1_XB + (p >> 1) * G1_UPOFF + 224 * (8 * g + q) + 8 * (p & 1) + wn * 7 * 16;
    const int wrd_e = wrd0 + 16 * bb, wrd_o = wrd0 - 16 * bb;

    f32x4 acc[7][4];
#pragma unroll
    for (int i = 0; i < 7; ++i)
#pragma unroll
        for (int j = 0; j < 4; ++j) acc[i][j] = f32x4{0, 0, 0, 0};

    stage(0, 0);
    stage(1, 1);
#pragma unroll 1
    for (int s = 0; s < 8; ++s) {
        if (s < 7) asm volatile("s_waitcnt vmcnt(11)" ::: "memory"); else asm volatile("s_waitcnt vmcnt(0)" ::: "memory");
        __builtin_amdgcn_s_barrier();
        if (s == 0) ST(1);
        const int buf = s % 3;
        if (s + 2 < 8) stage(s + 2, (s + 2) % 3);
        const char* base = smem + buf * G1_STAGE;
#pragma unroll
        for (int ks = 0; ks < 2; ++ks) {
            s16x8 xf[4];
#pragma unroll
            for (int mt = 0; mt < 4; ++mt) xf[mt] = *(const s16x8*)(base + xrd[ks] + mt * 2048);
#pragma unroll
            for (int i = 0; i < 7; ++i) {
                const int c8par = (7 * wn + i) & 1;
                const int a0 = (c8par ? wrd_o : wrd_e) + 16 * i + 224 * 32 * ks;
                s16x4 v0 = __builtin_amdgcn_ds_read_tr16_b64_v4i16((LAS s16x4*)(base + a0));
                s16x4 v1 = __builtin_amdgcn_ds_read_tr16_b64_v4i16((LAS s16x4*)(base + a0 + 224 * 4));
                s16x8 wf = __builtin_shufflevector(v0, v1, 0, 1, 2, 3, 4, 5, 6, 7);
#pragma unroll
                for (int mt = 0; mt < 4; ++mt) acc[i][mt] = mfma16(wf, xf[mt], acc[i][mt]);
            }
        }
        asm volatile("s_waitcnt lgkmcnt(0)" ::: "memory");
    }
    ST(2);
    unsigned hv[7][2][2];
#pragma unroll
    for (int i = 0; i < 7; ++i)
#pragma unroll
        for (int mp = 0; mp < 2; ++mp) {
            float hh[4];
#pragma unroll
            for (int r = 0; r < 4; ++r) {
                auto sw = __builtin_amdgcn_permlane32_swap(__float_as_uint(acc[i][2 * mp][r]), __float_as_uint(acc[i][2 * mp + 1][r]), false, false);
                const float gt = rbf(__uint_as_float(sw[0])), up = rbf(__uint_as_float(sw[1]));
                hh[r] = rbf(silu_f(gt)) * up;
            }
            hv[i][mp][0] = pk2(hh[0], hh[1]);
            hv[i][mp][1] = pk2(hh[2], hh[3]);
        }
    const int gg = (lane >> 4) & 1;
#pragma unroll
    for (int mp = 0; mp < 2; ++mp) {
        const int mtile = rb * 8 + wm * 4 + 2 * mp + (lane >> 5);
        char* hb = (char*)hf + (size_t)mtile * 56 * 1024;
        auto dst = [&](int c8) -> char* {
            const int G16 = 7 * cb + (c8 >> 1), kt = G16 >> 1, uu = G16 & 1;
            return hb + kt * 1024 + ((2 * uu + gg) * 16 + m) * 16;
        };
        if (wn == 0) {
#pragma unroll
            for (int i = 0; i < 6; i += 2) *(u32x4*)dst(i) = u32x4{hv[i][mp][0], hv[i][mp][1], hv[i + 1][mp][0], hv[i + 1][mp][1]};
            *(u32x2*)dst(6) = u32x2{hv[6][mp][0], hv[6][mp][1]};
        } else {
            *(u32x2*)(dst(7) + 8) = u32x2{hv[0][mp][0], hv[0][mp][1]};
#pragma unroll
            for (int i = 1; i < 7; i += 2) *(u32x4*)dst(7 + i) = u32x4{hv[i][mp][0], hv[i][mp][1], hv[i + 1][mp][0], hv[i + 1][mp][1]};
        }
    }
    ST(3);
    ST(5); ST_CLK(7); ST_FLUSH((char*)hf - WS_HF, 1);
}

constexpr int CV_KSTR = 4416;
constexpr int CV_KB = 4 * CV_KSTR;
constexpr int CV_UB = 96 * 80;
constexpr int CV_CH = CV_KB + CV_UB;
constexpr int CV_LDS = 2 * CV_CH;
constexpr int CV_QA = 48;

__device__ __forceinline__ void conv_role(int blk, const unsigned* __restrict__ xnp, const u16* __restrict__ kr, const u16* __restrict__ dskip,
                                          unsigned* __restrict__ sp, char* smem) {
    ST_DECL; ST(0); ST_CLK(6);
    const int tid = threadIdx.x, lane = tid & 63, wave = __builtin_amdgcn_readfirstlane(tid >> 6);
    {
        const unsigned* src = xnp + (size_t)blk * SL + tid * 8;
        const u32x4 a = *(const u32x4*)src, b = *(const u32x4*)(src + 4);
        u32x4 c0, c1;
        c0[0] = __builtin_amdgcn_perm(a[1], a[0], 0x05040100); c1[0] = __builtin_amdgcn_perm(a[1], a[0], 0x07060302);
        c0[1] = __builtin_amdgcn_perm(a[3], a[2], 0x05040100); c1[1] = __builtin_amdgcn_perm(a[3], a[2], 0x07060302);
        c0[2] = __builtin_amdgcn_perm(b[1], b[0], 0x05040100); c1[2] = __builtin_amdgcn_perm(b[1], b[0], 0x07060302);
        c0[3] = __builtin_amdgcn_perm(b[3], b[2], 0x05040100); c1[3] = __builtin_amdgcn_perm(b[3], b[2], 0x07060302);
        const int idx = 1024 + tid * 8;
        const int off = (idx >> 5) * 80 + (idx & 31) * 2;
        *(u32x4*)(smem + CV_KB + off) = c0;
        *(u32x4*)(smem + CV_CH + CV_KB + off) = c1;
        if (tid < 128) {
            const int zi = tid * 8, zo = (zi >> 5) * 80 + (zi & 31) * 2;
            *(u32x4*)(smem + CV_KB + zo) = u32x4{0, 0, 0, 0};
            *(u32x4*)(smem + CV_CH + CV_KB + zo) = u32x4{0, 0, 0, 0};
        }
    }
    for (int c = tid; c < 2 * (KR_LEN / 8); c += 256) {
        const int ch = c / (KR_LEN / 8), bq = c % (KR_LEN / 8);
        const u16* src = kr + (size_t)(blk * 2 + ch) * KR_LEN + bq * 8;
        const u32x4 own = *(const u32x4*)src;
        u32x4 prev = {0, 0, 0, 0};
        if (bq > 0) prev = *(const u32x4*)(src - 8);
        char* dst = smem + ch * CV_CH + bq * 16;
        *(u32x4*)(dst) = own;
        *(u32x4*)(dst + CV_KSTR) = u32x4{__builtin_amdgcn_alignbit(own[0], prev[3], 16), __builtin_amdgcn_alignbit(own[1], own[0], 16),
                                        __builtin_amdgcn_alignbit(own[2], own[1], 16), __builtin_amdgcn_alignbit(own[3], own[2], 16)};
        *(u32x4*)(dst + 2 * CV_KSTR) = u32x4{prev[3], own[0], own[1], own[2]};
        *(u32x4*)(dst + 3 * CV_KSTR) = u32x4{__builtin_amdgcn_alignbit(prev[3], prev[2], 16), __builtin_amdgcn_alignbit(own[0], prev[3], 16),
                                            __builtin_amdgcn_alignbit(own[1], own[0], 16), __builtin_amdgcn_alignbit(own[2], own[1], 16)};
    }
    __syncthreads();
    ST(1);
    const int ch = wave >> 1, wb = wave & 1;
    const char* kb = smem + ch * CV_CH;
    const char* ub = kb + CV_KB;
    const int r = lane & 31, h = lane >> 5;
    const int fa = (r & 3) * CV_KSTR + (KR_C - (r & ~3) + 8 * h) * 2;
    const int ua = (32 + r) * 80 + 16 * h;
    f32x16 acc0, acc1;
#pragma unroll
    for (int i = 0; i < 16; ++i) { acc0[i] = 0.f; acc1[i] = 0.f; }
    auto ldF = [&](int q) -> s16x8 {
        const char* p = kb + fa - 32 * q;
        const u32x2 lo = *(const u32x2*)p, hi = *(const u32x2*)(p + 8);
        return __builtin_bit_cast(s16x8, u32x4{lo[0], lo[1], hi[0], hi[1]});
    };
    auto ldB = [&](int o, int q) -> s16x8 {
        const int qp = (q + 2) / 2 - 1, qq = (q + 2) & 1;
        const int off = (32 * o - qp - qq) * 80 + 32 * qq;
        return *(const s16x8*)(ub + ua + off);
    };
    if (wb == 0) {
#pragma unroll 7
        for (int q = -1; q < CV_QA; ++q) {
            const s16x8 F = ldF(q);
            acc0 = mfma32(F, ldB(0, q), acc0);
            acc1 = mfma32(F, ldB(1, q), acc1);
        }
    } else {
#pragma unroll
        for (int q = CV_QA; q <= 64; ++q) {
            const s16x8 F = ldF(q);
            acc0 = mfma32(F, ldB(0, q), acc0);
            acc1 = mfma32(F, ldB(1, q), acc1);
        }
#pragma unroll 8
        for (int q = 65; q <= 128; ++q) {
            const s16x8 F = ldF(q);
            acc1 = mfma32(F, ldB(1, q), acc1);
        }
    }
    ST(2);
    __syncthreads();
    ST(3);
    {
        float* X = (float*)(smem + ch * CV_CH);
        const f32x16 send = wb ? acc0 : acc1;
#pragma unroll
        for (int i = 0; i < 4; ++i)
            *(f32x4*)(X + wb * 1024 + i * 256 + lane * 4) = f32x4{send[4 * i], send[4 * i + 1], send[4 * i + 2], send[4 * i + 3]};
        __syncthreads();
        f32x16 y = wb ? acc1 : acc0;
#pragma unroll
        for (int i = 0; i < 4; ++i) {
            const f32x4 o = *(const f32x4*)(X + (1 - wb) * 1024 + i * 256 + lane * 4);
#pragma unroll
            for (int j = 0; j < 4; ++j) y[4 * i + j] += o[j];
        }
        const float ds = bf2f(dskip[blk * 2 + ch]);
        u16* S = (u16*)(smem + ch * CV_CH + 8192);
#pragma unroll
        for (int i = 0; i < 4; ++i) {
            const int t = 1024 * wb + 32 * r + 8 * i + 4 * h;
            const int idx = 1024 + t;
            const u32x2 uu = *(const u32x2*)(ub + (idx >> 5) * 80 + (idx & 31) * 2);
            const float s0 = rbf(y[4 * i]) + rbf(bflo(uu[0]) * ds), s1 = rbf(y[4 * i + 1]) + rbf(bfhi(uu[0]) * ds);
            const float s2 = rbf(y[4 * i + 2]) + rbf(bflo(uu[1]) * ds), s3 = rbf(y[4 * i + 3]) + rbf(bfhi(uu[1]) * ds);
            *(u32x2*)(S + t) = u32x2{pk2(s0, s1), pk2(s2, s3)};
        }
    }
    __syncthreads();
    {
        const u16* S0 = (const u16*)(smem + 8192);
        const u16* S1 = (const u16*)(smem + CV_CH + 8192);
        const u32x4 a = *(const u32x4*)(S0 + tid * 8), b = *(const u32x4*)(S1 + tid * 8);
        u32x4 o0, o1;
        o0[0] = __builtin_amdgcn_perm(b[0], a[0], 0x05040100); o0[1] = __builtin_amdgcn_perm(b[0], a[0], 0x07060302);
        o0[2] = __builtin_amdgcn_perm(b[1], a[1], 0x05040100); o0[3] = __builtin_amdgcn_perm(b[1], a[1], 0x07060302);
        o1[0] = __builtin_amdgcn_perm(b[2], a[2], 0x05040100); o1[1] = __builtin_amdgcn_perm(b[2], a[2], 0x07060302);
        o1[2] = __builtin_amdgcn_perm(b[3], a[3], 0x05040100); o1[3] = __builtin_amdgcn_perm(b[3], a[3], 0x07060302);
        unsigned* dst = sp + (size_t)blk * SL + tid * 8;
        *(u32x4*)dst = o0;
        *(u32x4*)(dst + 4) = o1;
    }
    ST(5); ST_CLK(7); ST_FLUSH((char*)sp - WS_SP, 2);
}
__global__ __launch_bounds__(256, 2) void k_conv(const unsigned* __restrict__ xnp, const u16* __restrict__ kr, const u16* __restrict__ dskip, unsigned* __restrict__ sp) {
    extern __shared__ __attribute__((aligned(16))) char smem[];
    conv_role(blockIdx.x, xnp, kr, dskip, sp, smem);
}

constexpr int G2_LDS = 4 * 12 * 1024;
__global__ __launch_bounds__(256, 2) void k_gemm2(const u16* __restrict__ hf, const u16* __restrict__ wdf, const unsigned* __restrict__ sp,
                                                  const u16* __restrict__ x, u16* __restrict__ out) {
    extern __shared__ __attribute__((aligned(16))) char smem[];
    const int tid = threadIdx.x, lane = tid & 63, wave = __builtin_amdgcn_readfirstlane(tid >> 6);
    ST_DECL; ST(0); ST_CLK(6);
    const int b = blockIdx.x, xcd = b & 7, ix = b >> 3;
    const int rb = xcd * 4 + (ix & 3), cb = ix >> 2;
    const char* hb = (const char*)hf + ((size_t)(rb * 4) * 56 + wave * 14) * 1024 + lane * 16;
    const char* wb = (const char*)wdf + ((size_t)(cb * 4) * 56 + wave * 14) * 1024 + lane * 16;
    f32x4 acc[4][4];
#pragma unroll
    for (int i = 0; i < 4; ++i)
#pragma unroll
        for (int j = 0; j < 4; ++j) acc[i][j] = f32x4{0, 0, 0, 0};
    constexpr int PD = 3;
    s16x8 A[PD][4], B[PD][4];
    auto ld = [&](int kt, int sl) {
#pragma unroll
        for (int i = 0; i < 4; ++i) {
            A[sl][i] = *(const s16x8*)(wb + ((size_t)i * 56 + kt) * 1024);
            B[sl][i] = *(const s16x8*)(hb + ((size_t)i * 56 + kt) * 1024);
        }
    };
#pragma unroll
    for (int i = 0; i < PD - 1; ++i) { ld(i, i); __builtin_amdgcn_sched_barrier(0); }
#pragma unroll
    for (int kt = 0; kt < 14; ++kt) {
        if (kt + PD - 1 < 14) ld(kt + PD - 1, (kt + PD - 1) % PD);
        __builtin_amdgcn_sched_barrier(0);
#pragma unroll
        for (int i = 0; i < 4; ++i)
#pragma unroll
            for (int j = 0; j < 4; ++j) acc[i][j] = mfma16(A[kt % PD][i], B[kt % PD][j], acc[i][j]);
        __builtin_amdgcn_sched_barrier(0);
    }
    ST(2);
    f32x4* X = (f32x4*)smem;
#pragma unroll
    for (int j = 0; j < 4; ++j) {
        if (j != wave) {
            const int sl = wave - (wave > j ? 1 : 0);
#pragma unroll
            for (int i = 0; i < 4; ++i) X[((j * 3 + sl) * 4 + i) * 64 + lane] = acc[i][j];
        }
    }
    __syncthreads();
    f32x4 y[4];
#pragma unroll
    for (int i = 0; i < 4; ++i) {
        f32x4 own = wave == 0 ? acc[i][0] : (wave == 1 ? acc[i][1] : (wave == 2 ? acc[i][2] : acc[i][3]));
        f32x4 p[3];
#pragma unroll
        for (int s = 0; s < 3; ++s) p[s] = X[((wave * 3 + s) * 4 + i) * 64 + lane];
        y[i] = own + p[0] + p[1] + p[2];
    }
    const int m = lane & 15, g = lane >> 4;
    const int t = rb * 64 + wave * 16 + m;
#pragma unroll
    for (int i = 0; i < 4; ++i) {
        const int d = cb * 64 + 16 * i + 4 * g;
        const u32x2 xv = *(const u32x2*)(x + (size_t)t * DM + d);
        const unsigned s01 = sp[(size_t)(d >> 1) * SL + t], s23 = sp[(size_t)((d >> 1) + 1) * SL + t];
        const float o0 = bflo(xv[0]) + rbf(bflo(s01) * rbf(silu_f(rbf(y[i][0]))));
        const float o1 = bfhi(xv[0]) + rbf(bfhi(s01) * rbf(silu_f(rbf(y[i][1]))));
        const float o2 = bflo(xv[1]) + rbf(bflo(s23) * rbf(silu_f(rbf(y[i][2]))));
        const float o3 = bfhi(xv[1]) + rbf(bfhi(s23) * rbf(silu_f(rbf(y[i][3]))));
        *(u32x2*)(out + (size_t)t * DM + d) = u32x2{pk2(o0, o1), pk2(o2, o3)};
    }
    ST(5); ST_CLK(7); ST_FLUSH((char*)hf - WS_HF, 3);
}

extern "C" void kernel_launch(void* const* d_in, const int* in_sizes, int n_in, void* d_out, int out_size, void* d_ws, size_t ws_size,
                              hipStream_t stream) {
    char* ws = (char*)d_ws;
    PrepArgs pa;
    pa.x = (const u16*)d_in[0]; pa.Are = (const u16*)d_in[1]; pa.Aim = (const u16*)d_in[2]; pa.Bre = (const u16*)d_in[3];
    pa.Bim = (const u16*)d_in[4]; pa.Cre = (const u16*)d_in[5]; pa.Cim = (const u16*)d_in[6]; pa.logdt = (const u16*)d_in[7];
    pa.wd = (const u16*)d_in[12]; pa.ws = ws;
    (void)hipFuncSetAttribute((const void*)k_prep, hipFuncAttributeMaxDynamicSharedMemorySize, PREP_LDS);
    (void)hipFuncSetAttribute((const void*)k_gemm1, hipFuncAttributeMaxDynamicSharedMemorySize, G1_LDS);
    k_prep<<<NB_KG + NB_LN + NB_WD, 256, PREP_LDS, stream>>>(pa);
    k_gemm1<<<256, 256, G1_LDS, stream>>>((const u16*)(ws + WS_XN), (const u16*)d_in[10], (const u16*)d_in[11], (u16*)(ws + WS_HF));
    k_conv<<<256, 256, CV_LDS, stream>>>((const unsigned*)(ws + WS_XNP), (const u16*)(ws + WS_KR), (const u16*)d_in[8], (unsigned*)(ws + WS_SP));
    k_gemm2<<<256, 256, G2_LDS, stream>>>((const u16*)(ws + WS_HF), (const u16*)(ws + WS_WDF), (const unsigned*)(ws + WS_SP),
                                          (const u16*)d_in[0], (u16*)d_out);
}
```

```cpp
#define FUSED
#define PF 0
#include <hip/hip_runtime.h>
#include <stdint.h>
#include <type_traits>

#define GAS __attribute__((address_space(1)))
#define LAS __attribute__((address_space(3)))
typedef __attribute__((ext_vector_type(4))) short s16x4;
typedef __attribute__((ext_vector_type(8))) short s16x8;
typedef __attribute__((ext_vector_type(8))) __bf16 bf16x8;
typedef __attribute__((ext_vector_type(2))) __bf16 bf16x2;
typedef __attribute__((ext_vector_type(2))) float f32x2;
typedef __attribute__((ext_vector_type(4))) float f32x4;
typedef __attribute__((ext_vector_type(16))) float f32x16;
typedef __attribute__((ext_vector_type(4))) unsigned u32x4;
typedef __attribute__((ext_vector_type(2))) unsigned u32x2;
typedef unsigned short u16;

constexpr int SL = 2048, DM = 512, NS = 128, FF = 1792;

#define SB() __builtin_amdgcn_sched_barrier(0)
#define BAR() do { __builtin_amdgcn_s_barrier(); asm volatile("" ::: "memory"); } while (0)
#define LP(a) ((LAS char*)(size_t)(unsigned)(a))
template <int N> __device__ __forceinline__ void wait_vm() { asm volatile("s_waitcnt vmcnt(%0)" :: "n"(N) : "memory"); }
__device__ __forceinline__ void wait_lgkm0() { asm volatile("s_waitcnt lgkmcnt(0)" ::: "memory"); }
#define LBAR() do { wait_lgkm0(); BAR(); } while (0)

__device__ __forceinline__ float bf2f(u16 v) { return __uint_as_float(((unsigned)v) << 16); }
__device__ __forceinline__ float bflo(unsigned v) { return __uint_as_float(v << 16); }
__device__ __forceinline__ float bfhi(unsigned v) { return __uint_as_float(v & 0xffff0000u); }
__device__ __forceinline__ unsigned pk2(float a, float b) {
    f32x2 z = {a, b};
    return __builtin_bit_cast(unsigned, __builtin_convertvector(z, bf16x2));
}
__device__ __forceinline__ float rbf(float a) { return bflo(pk2(a, 0.f)); }
__device__ __forceinline__ float sigm(float x) { return __builtin_amdgcn_rcpf(1.f + __builtin_amdgcn_exp2f(x * -1.44269504088896341f)); }
__device__ __forceinline__ f32x4 mfma16(s16x8 a, s16x8 b, f32x4 c) {
    return __builtin_amdgcn_mfma_f32_16x16x32_bf16(__builtin_bit_cast(bf16x8, a), __builtin_bit_cast(bf16x8, b), c, 0, 0, 0);
}
__device__ __forceinline__ f32x16 mfma32(s16x8 a, s16x8 b, f32x16 c) {
    return __builtin_amdgcn_mfma_f32_32x32x16_bf16(__builtin_bit_cast(bf16x8, a), __builtin_bit_cast(bf16x8, b), c, 0, 0, 0);
}
__device__ __forceinline__ void set_m0(unsigned lds) { asm volatile("s_mov_b32 m0, %0" :: "s"(lds)); }
template <int I> __device__ __forceinline__ void glds_o(const char* g, unsigned off) {
    asm volatile("global_load_lds_dwordx4 %1, %0 offset:%2" :: "s"(g), "v"(off), "n"(I * 1024));
}

#define ST_DECL
#define ST(i)
#define ST_CLK(i)
#define ST_FLUSH(wsbase, kid)
#define ST_ARG
#define ST_PASS
constexpr size_t WS_FLAG  = 0;
constexpr size_t WS_STATS = 32768;
constexpr size_t WS_CS    = WS_STATS + 2048 * 8;
constexpr size_t WS_XNP   = 65536;
constexpr size_t WS_HF    = WS_XNP + (size_t)SL * DM * 2;
constexpr size_t WS_WDF   = WS_HF + (size_t)SL * FF * 2;
constexpr size_t WS_SP    = WS_WDF + (size_t)FF * DM * 2;
constexpr size_t WS_END   = WS_SP + (size_t)SL * DM * 2;
constexpr int FL_EPOCH = 0, FL_LN = 32, FL_CS = 64, FL_WD = 96, FL_H = 256  , FL_S = 512  , FL_ARR = 1024  ;

struct Args {
    const u16 *x, *Are, *Aim, *Bre, *Bim, *Cre, *Cim, *logdt, *dskip, *wg, *wu, *wd;
    u16* out;
    char* ws;
};
typedef __amdgpu_buffer_rsrc_t rsrc_t;
__device__ __forceinline__ rsrc_t ws_rsrc(const Args& A) { return __builtin_amdgcn_make_buffer_rsrc(A.ws, 0, (unsigned)WS_END, 0x00020000); }
constexpr int AUX_PUB = 16;
__device__ __forceinline__ void st_pub16(rsrc_t rs, unsigned off, u32x4 v) { __builtin_amdgcn_raw_buffer_store_b128(v, rs, off, 0, AUX_PUB); }
__device__ __forceinline__ void st_pub8(rsrc_t rs, unsigned off, u32x2 v) { __builtin_amdgcn_raw_buffer_store_b64(v, rs, off, 0, AUX_PUB); }
__device__ __forceinline__ u32x4 ld_pub16(rsrc_t rs, unsigned off, unsigned soff = 0) { return __builtin_amdgcn_raw_buffer_load_b128(rs, off, soff, AUX_PUB); }
__device__ __forceinline__ u32x2 ld_pub8(rsrc_t rs, unsigned off) { return __builtin_amdgcn_raw_buffer_load_b64(rs, off, 0, AUX_PUB); }

template <int CTRL> __device__ __forceinline__ float dpp_f(float x) {
    return __builtin_bit_cast(float, __builtin_amdgcn_mov_dpp(__builtin_bit_cast(int, x), CTRL, 0xf, 0xf, true));
}
__device__ __forceinline__ float row16_sum(float x) {
    x += dpp_f<0x128>(x); x += dpp_f<0x124>(x); x += dpp_f<0x122>(x); x += dpp_f<0x121>(x);
    return x;
}
__device__ __forceinline__ float xrow_sum(float x) {
    auto s = __builtin_amdgcn_permlane16_swap(__float_as_uint(x), __float_as_uint(x), false, false);
    x = __uint_as_float(s[0]) + __uint_as_float(s[1]);
    auto t = __builtin_amdgcn_permlane32_swap(__float_as_uint(x), __float_as_uint(x), false, false);
    return __uint_as_float(t[0]) + __uint_as_float(t[1]);
}
struct AuxX { u32x4 x[8]; };
struct AuxW { u32x4 w[16]; };
struct AuxD { u32x4 d[8]; };
__device__ __forceinline__ void aux_load_x(int a, const Args& A, AuxX& in) {
    const int tid = threadIdx.x, lane = tid & 63, wave = tid >> 6;
#pragma unroll
    for (int i = 0; i < 8; ++i)
        in.x[i] = *(const u32x4*)(A.x + (size_t)(a * 64 + wave * 8 + (i >> 2) * 4 + (lane >> 4)) * DM + ((i & 3) * 16 + (lane & 15)) * 8);
}
__device__ __forceinline__ void aux_load_w(int a, const Args& A, AuxW& in) {
    const int tid = threadIdx.x, c = tid & 7, r = tid >> 3;
#pragma unroll
    for (int i = 0; i < 16; ++i) in.w[i] = *(const u32x4*)(((i >> 3) ? A.wu : A.wg) + (size_t)(r + (i & 7) * 64) * FF + a * 64 + c * 8);
}
__device__ __forceinline__ void aux_load_d(int a, const Args& A, AuxD& in) {
    const int tid = threadIdx.x;
#pragma unroll
    for (int i = 0; i < 8; ++i) { const int cc = i * 512 + tid; in.d[i] = *(const u32x4*)(A.wd + (size_t)(a * 64 + (cc >> 6)) * DM + (cc & 63) * 8); }
}
__device__ __forceinline__ float dot2bf(unsigned a, unsigned b, float c) {
    return __builtin_amdgcn_fdot2_f32_bf16(__builtin_bit_cast(bf16x2, a), __builtin_bit_cast(bf16x2, b), c, false);
}
__device__ __forceinline__ void ln_part(int a, const Args& A, const AuxX& in, char* smem) {
    const int tid = threadIdx.x, lane = tid & 63, wave = tid >> 6;
    const rsrc_t rs = ws_rsrc(A);
#pragma unroll
    for (int ps = 0; ps < 2; ++ps) {
        float s0 = 0.f, s1 = 0.f, q0 = 0.f, q1 = 0.f;
#pragma unroll
        for (int i = 0; i < 4; ++i)
#pragma unroll
            for (int j = 0; j < 4; j += 2) {
                const unsigned u0 = in.x[4 * ps + i][j], u1 = in.x[4 * ps + i][j + 1];
                s0 = dot2bf(u0, 0x3f803f80u, s0); s1 = dot2bf(u1, 0x3f803f80u, s1);
                q0 = dot2bf(u0, u0, q0); q1 = dot2bf(u1, u1, q1);
            }
        const float mean = row16_sum(s0 + s1) * (1.f / DM);
        const float var = rbf(fmaxf(row16_sum(q0 + q1) * (1.f / DM) - mean * mean, 0.f));
        const float den = rbf(__builtin_sqrtf(rbf(var + 1e-5f)));
        const float mb = rbf(mean), rd = 1.f / den, nm = -mb * rd;
        const int r = wave * 8 + ps * 4 + (lane >> 4);
        if ((lane & 15) == 0) st_pub8(rs, WS_STATS + (a * 64 + r) * 8, u32x2{__float_as_uint(mb), __float_as_uint(rd)});
#pragma unroll
        for (int i = 0; i < 4; ++i) {
            u32x4 o;
#pragma unroll
            for (int j = 0; j < 4; ++j) {
                const unsigned u = in.x[4 * ps + i][j];
                const f32x2 v = __builtin_elementwise_fma(f32x2{bflo(u), bfhi(u)}, f32x2{rd, rd}, f32x2{nm, nm});
                o[j] = pk2(v[0], v[1]);
            }
            *(u32x4*)(smem + r * 1024 + (((i * 16 + (lane & 15)) ^ ((r >> 2) & 7)) << 4)) = o;
        }
    }
    LBAR();
    const unsigned* T = (const unsigned*)smem;
#pragma unroll
    for (int it = 0; it < 8; ++it) {
        const int dp = it * 32 + wave * 4 + (lane >> 4), tq = lane & 15;
        u32x4 o;
#pragma unroll
        for (int j = 0; j < 4; ++j) o[j] = T[(4 * tq + j) * 256 + (((dp >> 2) ^ (tq & 7)) << 2) + (dp & 3)];
        st_pub16(rs, WS_XNP + (dp * SL + a * 64 + 4 * tq) * 4, o);
    }
}
__device__ __forceinline__ void cs_part(int a, const Args& A, const AuxW& in, char* smem) {
    const int tid = threadIdx.x, lane = tid & 63, wave = tid >> 6;
    float* T = (float*)smem;
#pragma unroll
    for (int mat = 0; mat < 2; ++mat) {
        f32x2 s[4];
#pragma unroll
        for (int j = 0; j < 4; ++j) s[j] = f32x2{bflo(in.w[mat * 8][j]), bfhi(in.w[mat * 8][j])};
#pragma unroll
        for (int i = 1; i < 8; ++i)
#pragma unroll
            for (int j = 0; j < 4; ++j) s[j] += f32x2{bflo(in.w[mat * 8 + i][j]), bfhi(in.w[mat * 8 + i][j])};
        float r[8];
#pragma unroll
        for (int j = 0; j < 8; ++j) { r[j] = s[j >> 1][j & 1]; r[j] += dpp_f<0x128>(r[j]); r[j] = xrow_sum(r[j]); }
        if (lane < 8) {
            *(f32x4*)(T + (wave * 2 + mat) * 64 + lane * 8) = f32x4{r[0], r[1], r[2], r[3]};
            *(f32x4*)(T + (wave * 2 + mat) * 64 + lane * 8 + 4) = f32x4{r[4], r[5], r[6], r[7]};
        }
    }
    LBAR();
    if (tid < 64) {
        const int mat = tid >> 5, col = (tid & 31) * 2;
        float t0 = 0.f, t1 = 0.f;
#pragma unroll
        for (int w = 0; w < 8; ++w) { t0 += T[(w * 2 + mat) * 64 + col]; t1 += T[(w * 2 + mat) * 64 + col + 1]; }
        st_pub8(ws_rsrc(A), WS_CS + (mat * FF + a * 64 + col) * 4, u32x2{__float_as_uint(t0), __float_as_uint(t1)});
    }
}
__device__ __forceinline__ void wd_part(int a, const Args& A, const AuxD& in, char* smem) {
    const int tid = threadIdx.x;
#pragma unroll
    for (int i = 0; i < 8; ++i) {
        const int c = i * 512 + tid, r = c >> 6, cc = c & 63;
        *(u32x4*)(smem + r * 1024 + ((((cc >> 1) ^ (r & 7)) << 1) | (cc & 1)) * 16) = in.d[i];
    }
    LBAR();
    const rsrc_t rs = ws_rsrc(A);
    const int lane = tid & 63, wave = tid >> 6, g = lane >> 4, q = (lane & 15) >> 2, p = lane & 3;
    const unsigned lds0 = (unsigned)(size_t)(LAS char*)smem;
#pragma unroll
    for (int i = 0; i < 8; ++i) {
        const int f = i * 8 + wave, kk = f >> 5, dt = f & 31;
        const int r0 = 32 * kk + 4 * g + q, r1 = r0 + 16;
        const s16x4 lo = __builtin_amdgcn_ds_read_tr16_b64_v4i16((LAS s16x4*)LP(lds0 + r0 * 1024 + ((dt ^ (r0 & 7)) << 5) + p * 8));
        const s16x4 hi = __builtin_amdgcn_ds_read_tr16_b64_v4i16((LAS s16x4*)LP(lds0 + r1 * 1024 + ((dt ^ (r1 & 7)) << 5) + p * 8));
        st_pub16(rs, WS_WDF + ((dt * 56 + 2 * a + kk) * 64 + lane) * 16, __builtin_bit_cast(u32x4, __builtin_shufflevector(lo, hi, 0, 1, 2, 3, 4, 5, 6, 7)));
    }
}

typedef f32x2 cpx;
__device__ __forceinline__ cpx cmul(cpx a, cpx b) {
    cpx r;
    asm("v_pk_mul_f32 %0, %1, %2 op_sel:[0,0] op_sel_hi:[0,1]\n\t"
        "v_pk_fma_f32 %0, %1, %2, %0 op_sel:[1,1,0] op_sel_hi:[1,0,1] neg_lo:[1,0,0]" : "=&v"(r) : "v"(a), "v"(b));
    return r;
}
__device__ __forceinline__ cpx cexp_p(float are2, float airev, float p) {
    const float er = __builtin_amdgcn_exp2f(are2 * p);
    const float rv = __builtin_amdgcn_fractf(airev * p);
    return cpx{er * __builtin_amdgcn_cosf(rv), er * __builtin_amdgcn_sinf(rv)};
}
struct KgIn { u16 are, aim, bre, bim, cre, cim, ldt; };
__device__ __forceinline__ KgIn kg_load(int dp, const Args& A) {
    const int tid = threadIdx.x, lane = tid & 63, wave = tid >> 6;
    const int d = dp * 2 + ((wave >> 1) & 1), i = d * NS + (wave & 1) * 64 + lane;
    KgIn k;
    k.are = A.Are[i]; k.aim = A.Aim[i]; k.bre = A.Bre[i]; k.bim = A.Bim[i]; k.cre = A.Cre[i]; k.cim = A.Cim[i]; k.ldt = A.logdt[d];
    return k;
}
constexpr int KG_TAB = 16 * 81 * 16;
constexpr int KC_OFF = 88 * 1024;
__device__ __forceinline__ void cmul2(cpx& r0, cpx a0, cpx b0, cpx& r1, cpx a1, cpx b1) {
    asm("v_pk_mul_f32 %0, %2, %3 op_sel:[0,0] op_sel_hi:[0,1]\n\t"
        "v_pk_mul_f32 %1, %4, %5 op_sel:[0,0] op_sel_hi:[0,1]\n\t"
        "v_pk_fma_f32 %0, %2, %3, %0 op_sel:[1,1,0] op_sel_hi:[1,0,1] neg_lo:[1,0,0]\n\t"
        "v_pk_fma_f32 %1, %4, %5, %1 op_sel:[1,1,0] op_sel_hi:[1,0,1] neg_lo:[1,0,0]"
        : "=&v"(r0), "=&v"(r1) : "v"(a0), "v"(b0), "v"(a1), "v"(b1));
}
struct KgVals { unsigned v[41]; };
__device__ __forceinline__ void kgen_values(const KgIn& in, KgVals& o) {
    const int wave = __builtin_amdgcn_readfirstlane(threadIdx.x >> 6);
    const float dt = rbf(expf(bf2f(in.ldt)));
    const float are2 = dt * bf2f(in.are) * 1.44269504088896341f, airev = dt * bf2f(in.aim) * 0.15915494309189535f;
    auto put = [&](int i, cpx v) { o.v[i] = pk2(v[0], v[1]); };
    if (wave < 4) {
        cpx P[16];
        P[0] = cpx{1.f, 0.f};
        P[1] = cexp_p(are2, -airev, 1.f);
        P[2] = cmul(P[1], P[1]);
        cmul2(P[3], P[2], P[1], P[4], P[2], P[2]);
        cmul2(P[5], P[4], P[1], P[6], P[3], P[3]);
        cmul2(P[7], P[6], P[1], P[8], P[4], P[4]);
        cmul2(P[9], P[8], P[1], P[10], P[5], P[5]);
        cmul2(P[11], P[10], P[1], P[12], P[6], P[6]);
        cmul2(P[13], P[12], P[1], P[14], P[7], P[7]);
        P[15] = cmul(P[14], P[1]);
#pragma unroll
        for (int b = 0; b < 16; ++b) put(b, P[b]);
#pragma unroll
        for (int s = 0; s < 3; ++s)
#pragma unroll
            for (int i = 0; i < 8; i += 2) {
                cpx a0 = P[8 + i], a1 = P[9 + i];
                cmul2(P[8 + i], a0, a0, P[9 + i], a1, a1);
                put(16 + 8 * s + i, P[8 + i]); put(17 + 8 * s + i, P[9 + i]);
            }
        o.v[40] = 0;
    } else {
        const cpx dB = {dt * bf2f(in.bre), dt * bf2f(in.bim)};
        const cpx w = cmul(cpx{bf2f(in.cre), bf2f(in.cim)}, dB);
        const cpx z16 = cexp_p(are2, airev, 16.f), z128 = cexp_p(are2, airev, 128.f);
        cpx z32, z64, z256, z512, p0, p1;
        cmul2(z32, z16, z16, z256, z128, z128);
        cmul2(z64, z32, z32, z512, z256, z256);
        const cpx z1024 = cmul(z512, z512);
        p0 = w; p1 = cmul(w, z16);
#pragma unroll
        for (int a = 0; a < 16; a += 2) {
            put(a, p0); put(1 + a, p1);
            if (a < 14) { cpx q0 = p0, q1 = p1; cmul2(p0, q0, z32, p1, q1, z32); }
        }
        cmul2(p0, w, z256, p1, w, z512);
#pragma unroll
        for (int a = 0; a < 8; ++a) {
            put(16 + a, p0); put(24 + a, p1);
            if (a < 7) { cpx q0 = p0, q1 = p1; cmul2(p0, q0, z32, p1, q1, z64); }
        }
        p0 = cmul(w, z1024); p1 = cmul(p0, z128);
#pragma unroll
        for (int a = 0; a < 8; a += 2) {
            put(32 + a, p0); put(33 + a, p1);
            cpx q0 = p0, q1 = p1; cmul2(p0, q0, z256, p1, q1, z256);
        }
        put(40, p0);
    }
#pragma unroll
    for (int i = 0; i < 41; ++i) asm volatile("" : "+v"(o.v[i]));
}
__device__ __forceinline__ void kgen_tables(const KgVals& in, char* smem) {
    const int tid = threadIdx.x, lane = tid & 63, wave = __builtin_amdgcn_readfirstlane(tid >> 6);
    unsigned* tab = (unsigned*)(smem + (wave & 3) * KG_TAB) + (lane >> 2) * (81 * 4) + (lane & 3) + (wave >> 2) * (40 * 4);
#pragma unroll
    for (int i = 0; i < 40; ++i) tab[i * 4] = in.v[i];
    if (wave >= 4) tab[40 * 4] = in.v[40];
    LBAR();
    {
        const int ch = wave >> 2, s = wave & 3, b = lane & 15, g = lane >> 4;
        const int ra = s == 0 ? 40 + b : (s == 3 ? 72 + min(b, 8) : 48 + 8 * s + (b & 7));
        const int sh = min(s, b == 0 ? 3 : __builtin_clz((unsigned)b) - 28), bb = b << sh, e = s - sh;
        const int rb = e > 0 ? 16 + 8 * (e - 1) + (bb - 8) : bb;
        const char* ta = smem + ch * 2 * KG_TAB + (g * 81 + ra) * 16;
        const char* tb = smem + ch * 2 * KG_TAB + (g * 81 + rb) * 16;
        f32x4 acc = {0, 0, 0, 0};
#pragma unroll
        for (int k = 0; k < 8; ++k)
            acc = mfma16(*(const s16x8*)(ta + (k >> 2) * KG_TAB + (k & 3) * (4 * 81 * 16)), *(const s16x8*)(tb + (k >> 2) * KG_TAB + (k & 3) * (4 * 81 * 16)), acc);
        float* kc = (float*)(smem + KC_OFF) + ch * 656 + (s ? 128 * (s + 1) : 0) + 64 * g + b;
        if (s == 0 || g < 2) { kc[0] = acc[0]; kc[16] = acc[1]; kc[32] = acc[2]; kc[48] = acc[3]; }
        else if (s == 3 && g == 2) kc[0] = acc[0];
    }
}

constexpr int KR_C = 2087;
constexpr int CV_KSTR = 4416;
constexpr int CV_KB = 4 * CV_KSTR;
constexpr int CV_UB = 96 * 80;
constexpr int CV_U = 36 * 1024;
constexpr int CV_X = 52 * 1024;
constexpr int G2_S = 52 * 1024;
constexpr int G2_P = 94 * 1024;
constexpr int CTL_OFF = 158 * 1024;
struct CvIn { u32x4 u; u16 ds; };
__device__ __forceinline__ void conv_uimage(const CvIn& in, char* smem) {
    const int tid = threadIdx.x;
    const int idx = 1024 + tid * 4, off = (idx >> 5) * 80 + (idx & 31) * 2;
    *(u32x2*)(smem + CV_U + off) = u32x2{__builtin_amdgcn_perm(in.u[1], in.u[0], 0x05040100), __builtin_amdgcn_perm(in.u[3], in.u[2], 0x05040100)};
    *(u32x2*)(smem + CV_U + CV_UB + off) = u32x2{__builtin_amdgcn_perm(in.u[1], in.u[0], 0x07060302), __builtin_amdgcn_perm(in.u[3], in.u[2], 0x07060302)};
    if (tid < 256) {
        const int ch = tid >> 7, zi = (tid & 127) * 8;
        *(u32x4*)(smem + CV_U + ch * CV_UB + (zi >> 5) * 80 + (zi & 31) * 2) = u32x4{0, 0, 0, 0};
    }
}
struct KIdx { unsigned p[6]; };
__device__ __forceinline__ void conv_kindex(KIdx& o) {
    const int i = threadIdx.x & 255, top = 2050 - 8 * i;
#pragma unroll
    for (int e = 0; e < 12; e += 2) {
        const int l0 = min(top - e, SL - 1), l1 = min(max(top - e - 1, 0), SL - 1);
        const unsigned bb = pk2((float)l0, (float)l1);
        const unsigned i0 = l0 < 128 ? l0 : (bb & 0xffffu) - 17024u, i1 = l1 < 128 ? l1 : (bb >> 16) - 17024u;
        o.p[e >> 1] = (i0 << 2) | (i1 << 18);
        asm volatile("" : "+v"(o.p[e >> 1]));
    }
}
__device__ __forceinline__ void conv_kimage(const KIdx& ix, char* smem) {
    const int tid = threadIdx.x, ch = tid >> 8, i = tid & 255;
    const char* kc = smem + KC_OFF + ch * 656 * 4;
    float v[11];
#pragma unroll
    for (int e = 0; e < 12; e += 2) {
        v[e] = *(const float*)(kc + (ix.p[e >> 1] & 0xffffu));
        if (e + 1 < 11) v[e + 1] = *(const float*)(kc + (ix.p[e >> 1] >> 16));
    }
    if (i == 0) { v[0] = 0.f; v[1] = 0.f; v[2] = 0.f; }
    unsigned ev[5], od[5];
#pragma unroll
    for (int k = 0; k < 5; ++k) { ev[k] = pk2(v[2 * k], v[2 * k + 1]); od[k] = pk2(v[2 * k + 1], v[2 * k + 2]); }
    char* dst = smem + ch * CV_KB + (i + 5) * 16;
    *(u32x4*)(dst) = u32x4{od[1], od[2], od[3], od[4]};
    *(u32x4*)(dst + CV_KSTR) = u32x4{ev[1], ev[2], ev[3], ev[4]};
    *(u32x4*)(dst + 2 * CV_KSTR) = u32x4{od[0], od[1], od[2], od[3]};
    *(u32x4*)(dst + 3 * CV_KSTR) = u32x4{ev[0], ev[1], ev[2], ev[3]};
    if (i == 255) {
        char* e = smem + ch * CV_KB + 261 * 16;
        *(u32x4*)(e) = u32x4{0, 0, 0, 0};
        *(u32x4*)(e + CV_KSTR) = u32x4{pk2(v[10], 0.f), 0, 0, 0};
        *(u32x4*)(e + 2 * CV_KSTR) = u32x4{od[4], 0, 0, 0};
        *(u32x4*)(e + 3 * CV_KSTR) = u32x4{ev[4], pk2(v[10], 0.f), 0, 0};
    }
    if (i < 32) *(u32x4*)(smem + ch * CV_KB + (i >> 3) * CV_KSTR + ((i & 7) < 5 ? (i & 7) : 257 + (i & 7)) * 16) = u32x4{0, 0, 0, 0};
}
template <int Q0, int Q1, int NTILE>
__device__ __forceinline__ void conv_span(unsigned fa, unsigned ua, f32x16& acc0, f32x16& acc1) {
    constexpr int N = Q1 - Q0 + 1, PD = NTILE == 2 ? 4 : 6;
    const unsigned fb = fa - 32 * Q1, ub = ua - ((Q1 + 1) / 2) * 80;
    unsigned fb2 = fb + 8;
    asm volatile("" : "+v"(fb2));
    u32x2 flo[PD], fhi[PD];
    s16x8 b0[PD], b1[PD];
    auto ld = [&](int q, int sl) {
        const int fo = 32 * (Q1 - q);
        flo[sl] = *(const LAS u32x2*)(LP(fb) + fo); fhi[sl] = *(const LAS u32x2*)(LP(fb2) + fo);
        const int qq = q & 1, blk = (q + qq) / 2;
        const int uo = ((Q1 + 1) / 2 - blk) * 80 + 32 * qq;
        if (NTILE == 2) b0[sl] = *(const LAS s16x8*)(LP(ub) + uo);
        b1[sl] = *(const LAS s16x8*)(LP(ub) + uo + 32 * 80);
    };
#pragma unroll
    for (int i = 0; i < PD; ++i) if (i < N) ld(Q0 + i, i);
    SB();
#pragma unroll
    for (int i = 0; i < N; ++i) {
        const int sl = i % PD;
        const s16x8 F = __builtin_bit_cast(s16x8, u32x4{flo[sl][0], flo[sl][1], fhi[sl][0], fhi[sl][1]});
        const s16x8 x0 = b0[sl], x1 = b1[sl];
        if (NTILE == 2) acc0 = mfma32(F, x0, acc0);
        acc1 = mfma32(F, x1, acc1);
        if (i + PD < N) ld(Q0 + i + PD, sl);
        SB();
    }
}
__device__ __forceinline__ void conv_mma(int wave, char* smem, f32x16& acc0, f32x16& acc1) {
    const int lane = threadIdx.x & 63;
    const unsigned lds0 = (unsigned)(size_t)(LAS char*)smem;
    const int ch = (wave >> 1) & 1, part = (wave < 4 ? 2 : 0) + (wave & 1);
    const int r = lane & 31, h = lane >> 5, S = (r + 1) & 3;
    const unsigned fa = lds0 + ch * CV_KB + S * CV_KSTR + (KR_C - r + 8 * h + S) * 2;
    const unsigned ua = lds0 + CV_U + ch * CV_UB + (32 + r) * 80 + 16 * h;
#pragma unroll
    for (int i = 0; i < 16; ++i) { acc0[i] = 0.f; acc1[i] = 0.f; }
    if (part == 0) conv_span<-1, 23, 2>(fa, ua, acc0, acc1);
    else if (part == 1) conv_span<24, 47, 2>(fa, ua, acc0, acc1);
    else if (part == 2) { conv_span<48, 64, 2>(fa, ua, acc0, acc1); conv_span<65, 79, 1>(fa, ua, acc0, acc1); }
    else conv_span<80, 128, 1>(fa, ua, acc0, acc1);
}
__device__ __forceinline__ unsigned lds_ld(unsigned addr) { return __hip_atomic_load((LAS unsigned*)LP(addr), __ATOMIC_RELAXED, __HIP_MEMORY_SCOPE_WORKGROUP); }
__device__ __forceinline__ void lds_st(unsigned addr, unsigned v) { __hip_atomic_store((LAS unsigned*)LP(addr), v, __ATOMIC_RELAXED, __HIP_MEMORY_SCOPE_WORKGROUP); }
__device__ __forceinline__ unsigned lds_add(unsigned addr) { return __hip_atomic_fetch_add((LAS unsigned*)LP(addr), 1u, __ATOMIC_RELAXED, __HIP_MEMORY_SCOPE_WORKGROUP); }
__device__ __forceinline__ void put_tile(char* slot, const f32x16& a) {
    f32x4* X = (f32x4*)slot + (threadIdx.x & 63);
#pragma unroll
    for (int i = 0; i < 4; ++i) X[i * 64] = f32x4{a[4 * i], a[4 * i + 1], a[4 * i + 2], a[4 * i + 3]};
}
__device__ __forceinline__ void add_tile(f32x16& y, const char* slot) {
    const f32x4* X = (const f32x4*)slot + (threadIdx.x & 63);
#pragma unroll
    for (int i = 0; i < 4; ++i) {
        const f32x4 p = X[i * 64];
#pragma unroll
        for (int e = 0; e < 4; ++e) y[4 * i + e] += p[e];
    }
}
__device__ __forceinline__ void conv_handover(int wave, char* smem, const f32x16& acc0, const f32x16& acc1) {
    const unsigned lds0 = (unsigned)(size_t)(LAS char*)smem;
    const int ch = (wave >> 1) & 1;
    char* X = smem + CV_X + ch * 20480;
    if (wave >= 4) { put_tile(X + (wave & 1) * 4096, acc0); put_tile(X + (2 + (wave & 1)) * 4096, acc1); }
    else if ((wave & 1) == 0) put_tile(X + 4 * 4096, acc1);
    if (wave >= 4 || (wave & 1) == 0) {
        wait_lgkm0();
        if ((threadIdx.x & 63) == 0) lds_add(lds0 + CTL_OFF + 8 + ch * 4);
    }
}
__device__ __forceinline__ void conv_finish(int wave, int dp, const Args& A, u16 dsk, char* smem, const f32x16& acc0, const f32x16& acc1) {
    const int lane = threadIdx.x & 63;
    const unsigned lds0 = (unsigned)(size_t)(LAS char*)smem;
    const int ch = (wave >> 1) & 1, o = wave & 1, r = lane & 31, h = lane >> 5;
    while (__builtin_amdgcn_readfirstlane(lds_ld(lds0 + CTL_OFF + 8 + ch * 4)) < 3) __builtin_amdgcn_s_sleep(1);
    asm volatile("" ::: "memory");
    const char* X = smem + CV_X + ch * 20480;
    f32x16 y;
    if (o == 0) { y = acc0; add_tile(y, X); add_tile(y, X + 4096); }
    else { y = acc1; add_tile(y, X + 2 * 4096); add_tile(y, X + 3 * 4096); add_tile(y, X + 4 * 4096); }
    const float ds = bf2f(dsk);
    char* S = smem + CV_X + ch * 20480 + o * 2 * 4096;
    const char* U = smem + CV_U + ch * CV_UB + (32 + 32 * o + r) * 80 + 8 * h;
#pragma unroll
    for (int i = 0; i < 4; ++i) {
        const u32x2 uu = *(const u32x2*)(U + 16 * i);
        const f32x2 y0 = f32x2{y[4 * i], y[4 * i + 1]} + f32x2{bflo(uu[0]), bfhi(uu[0])} * ds;
        const f32x2 y1 = f32x2{y[4 * i + 2], y[4 * i + 3]} + f32x2{bflo(uu[1]), bfhi(uu[1])} * ds;
        *(u32x2*)(S + r * 64 + ((i ^ ((r >> 1) & 3)) << 4) + h * 8) = u32x2{pk2(y0[0], y0[1]), pk2(y1[0], y1[1])};
    }
    wait_lgkm0();
    const rsrc_t rs = ws_rsrc(A);
#pragma unroll
    for (int i = 0; i < 2; ++i)
        st_pub16(rs, WS_SP + ((dp * 2 + ch) * SL + 1024 * o) * 2 + i * 1024 + lane * 16, *(const u32x4*)(S + i * 1024 + (lane >> 2) * 64 + (((lane & 3) ^ ((lane >> 3) & 3)) << 4)));
}
typedef unsigned long long u64;
__device__ __forceinline__ void st_flag(u64* p, u64 v) { __hip_atomic_store((GAS u64*)p, v, __ATOMIC_RELAXED, __HIP_MEMORY_SCOPE_AGENT); }
__device__ __forceinline__ u64 ld_flag(const u64* p) { return __hip_atomic_load((GAS u64*)p, __ATOMIC_RELAXED, __HIP_MEMORY_SCOPE_AGENT); }
#define DBG_TMO(code)
#define DBG_ARG
#define DBG_PASS(c)
template <int SLEEP = 4>
__device__ __forceinline__ void wait_flags(const u64* p, u64 tag DBG_ARG) {
    const u64 t0 = __builtin_amdgcn_s_memrealtime();
    while (!__all(ld_flag(p) == tag)) {
        if (__builtin_amdgcn_s_memrealtime() - t0 > 300000ull) { DBG_TMO(code); break; }
        __builtin_amdgcn_s_sleep(SLEEP);
    }
    asm volatile("" ::: "memory");
}
template <int SLEEP = 4>
__device__ __forceinline__ void team_wait_flags(bool poller, unsigned lds_word, const u64* p, u64 tag DBG_ARG) {
    if (poller) {
        wait_flags<SLEEP>(p, tag);
        if ((threadIdx.x & 63) == 0) lds_st(lds_word, 1u);
    } else {
        while (__builtin_amdgcn_readfirstlane(lds_ld(lds_word)) == 0) __builtin_amdgcn_s_sleep(4);
        asm volatile("" ::: "memory");
    }
}
__device__ __forceinline__ void poll_issue(const u64* p) { asm volatile("global_load_dwordx2 v[254:255], %0, off sc1" :: "v"(p) : "v254", "v255"); }
template <int SLEEP = 4>
__device__ __forceinline__ void poll_check(const u64* p, u64 tag DBG_ARG) {
    unsigned lo, hi;
    asm volatile("s_waitcnt vmcnt(0)\n\tv_mov_b32 %0, v254\n\tv_mov_b32 %1, v255" : "=v"(lo), "=v"(hi) :: "memory");
    const u64 v = ((u64)hi << 32) | lo;
    if (!__all(v == tag)) wait_flags<SLEEP>(p, tag);
}
template <int OFF> __device__ __forceinline__ void glds_dw(const void* p) {
    asm volatile("global_load_lds_dword %0, off offset:%1" :: "v"((const char*)p - OFF), "n"(OFF));
}
template <int OFF> __device__ __forceinline__ void glds_dw_sc1(const void* p) {
    asm volatile("global_load_lds_dword %0, off offset:%1 sc1" :: "v"((const char*)p - OFF), "n"(OFF));
}
constexpr int DUMMY_OFF = CTL_OFF + 512;
__device__ __forceinline__ void touch(unsigned lds0, const void* p) { set_m0(lds0 + DUMMY_OFF); asm volatile("s_nop 0"); glds_dw_sc1<0>(p); }
__device__ __forceinline__ unsigned kg_area(int wave) { return 32 * 1024 + wave * 2048; }
__device__ __forceinline__ void kg_issue(int dp, const Args& A, unsigned lds0) {
    const int tid = threadIdx.x, lane = tid & 63, wave = __builtin_amdgcn_readfirstlane(tid >> 6);
    const int d = dp * 2 + ((wave >> 1) & 1), i = d * NS + 2 * lane;
    set_m0(lds0 + kg_area(wave));
    asm volatile("s_nop 0");
    glds_dw<0>(A.Are + i); glds_dw<256>(A.Aim + i); glds_dw<512>(A.Bre + i); glds_dw<768>(A.Bim + i);
    glds_dw<1024>(A.Cre + i); glds_dw<1280>(A.Cim + i); glds_dw<1536>(A.logdt + (d & ~1));
    glds_dw_sc1<7 * 256>((const char*)(A.ws + WS_FLAG) + FL_EPOCH * 8 + (lane & 1) * 4);
}
__device__ __forceinline__ KgIn kg_read(const char* smem, u64& tag) {
    const int tid = threadIdx.x, lane = tid & 63, wave = __builtin_amdgcn_readfirstlane(tid >> 6);
    const char* ar = smem + kg_area(wave);
    const u16* pm = (const u16*)ar + (wave & 1) * 64 + lane;
    KgIn r;
    r.are = pm[0]; r.aim = pm[128]; r.bre = pm[256]; r.bim = pm[384]; r.cre = pm[512]; r.cim = pm[640];
    r.ldt = ((const u16*)(ar + 1536))[(wave >> 1) & 1];
    tag = *(const u64*)(ar + 1792) + 1;
    wait_lgkm0();
    return r;
}

constexpr int XS = 16384, WSL = 16384, NXS = 3, NWS = 6;
constexpr int CS_OFF = CTL_OFF + 1024;
constexpr int FUSED_LDS = 160 * 1024;
#define SWAIT0 48
#define SWAIT1 24
#define CVPRIO 2
#define CSM 1
#define NO_CS 0
#define G2PD 5

__global__ __launch_bounds__(512, 2) void k_fused(Args A) {
    extern __shared__ __attribute__((aligned(16))) char smem[];
    ST_DECL; ST(0);
    const int tid = threadIdx.x, lane = tid & 63, wave = __builtin_amdgcn_readfirstlane(tid >> 6);
    const int b = blockIdx.x, xcd = b & 7, ix = b >> 3;
    const int dp = b;
    const int rb2 = xcd * 4 + (ix & 3), cb2 = ix >> 2;
    u64* fl = (u64*)(A.ws + WS_FLAG);
    const rsrc_t rs = ws_rsrc(A);
    const unsigned lds0 = (unsigned)(size_t)(LAS char*)smem;
    u64 tag;
    CvIn cin;

    KgVals kv;
    KIdx kix;
    if (ix < 28) {
        const int rb = (xcd & 3) * 4 + (ix & 3), cb = (xcd >> 2) * 7 + (ix >> 2);
        const int wm = wave & 1, wn = wave >> 1;
        unsigned xoff[2], woff[2];
#pragma unroll
        for (int i = 0; i < 2; ++i) {
            const int ci = (wave * 2 + i) * 64 + lane, row = ci >> 3, pos = ci & 7, ch = pos ^ ((row >> 1) & 7);
            xoff[i] = ((rb * 128 + row) * DM + ch * 8) * 2 - i * 1024 + 4096;
        }
#pragma unroll
        for (int i = 0; i < 2; ++i) {
            const int ci = ((wave & 3) * 2 + i) * 64 + lane, k = ci >> 4, pos = ci & 15;
            const int f = (k & 3) | (((k >> 3) & 1) << 2);
            const int c = (((pos >> 1) ^ f) << 1) | (pos & 1);
            woff[i] = (k * FF + cb * 128 + c * 8) * 2 - i * 1024 + 4096;
        }
        const char* xb = (const char*)A.x - 4096;
        const char* wb = (const char*)((wave >> 2) ? A.wu : A.wg) - 4096;
        const unsigned xdst = lds0 + wave * 2048, wdst = lds0 + NXS * XS + wave * 2048;
        auto m0x = [&](int X) { set_m0(xdst + (X % NXS) * XS); };
        auto m0w = [&](int j) { set_m0(wdst + (j % NWS) * WSL); };
#define LDX(X, I) glds_o<I>(xb + (X) * 128, xoff[I])
#define LDW(J, I) glds_o<I>(wb + (size_t)(J) * (32 * FF * 2), woff[I])
#define LDX2(X) do { m0x(X); asm volatile("s_nop 0"); LDX(X, 0); LDX(X, 1); } while (0)
#define LDW2(J) do { m0w(J); asm volatile("s_nop 0"); LDW(J, 0); LDW(J, 1); } while (0)
        LDX2(0); LDW2(0);
        kg_issue(dp, A, lds0);
        LDW2(1); LDX2(1); LDW2(2); LDW2(3); LDW2(4);
        SB();
        conv_kindex(kix);
        const int m = lane & 15, g = lane >> 4, q = (lane & 15) >> 2, p = lane & 3;
        const int fx = (m >> 1) & 7;
        int xrd[2];
#pragma unroll
        for (int ks = 0; ks < 2; ++ks) xrd[ks] = lds0 + (wm * 64 + m) * 128 + (((4 * ks + g) ^ fx) << 4);
        const int fw = q | ((g & 1) << 2);
        int wrd[2][4];
#pragma unroll
        for (int f = 0; f < 4; ++f) {
            wrd[0][f] = lds0 + NXS * XS + (8 * g + q) * 256 + (((wn * 2 + (f & 1)) ^ fw) << 5) + p * 8 + ((f >> 1) ^ wm) * 8192;
            wrd[1][f] = wrd[0][f] + 3 * WSL;
        }
        const char* pfa;
        if (tid < 352) { const int idx = (ix & 3) * 352 + tid, mat = idx >= 704, rem = idx - 704 * mat;
            pfa = (const char*)(mat ? A.wu : A.wg) + (size_t)(160 + (rem >> 1)) * (FF * 2) + cb * 256 + (rem & 1) * 128; }
        else { const int idx = min((ix >> 2) * 110 + min(tid - 352, 109), 767), row = (idx * 171) >> 10;
            pfa = (const char*)A.x + (size_t)(rb * 128 + row) * 1024 + (2 + idx - 6 * row) * 128; }
        wait_vm<10>();
        {
            const KgIn kin = kg_read(smem, tag);
            ST(1);
            kgen_values(kin, kv);
        }
        f32x4 acc[4][4];
#pragma unroll
        for (int i = 0; i < 4; ++i)
#pragma unroll
            for (int j = 0; j < 4; ++j) acc[i][j] = f32x4{0, 0, 0, 0};
        f32x4 csa[2] = {f32x4{0, 0, 0, 0}, f32x4{0, 0, 0, 0}};
        const s16x8 ones = {0x3f80, 0x3f80, 0x3f80, 0x3f80, 0x3f80, 0x3f80, 0x3f80, 0x3f80};
        s16x8 xf[2][4];
        s16x4 wlo[4], whi[4];
        auto rdx = [&](int buf, int j, int mt) { xf[buf][mt] = *(const LAS s16x8*)(LP(xrd[j & 1]) + ((j >> 1) % NXS) * XS + mt * 2048); };
        auto rdwl = [&](int j, int f) {
            const int slot = j % NWS;
            wlo[f] = __builtin_amdgcn_ds_read_tr16_b64_v4i16((LAS s16x4*)(LP(wrd[slot >= 3][f]) + (slot % 3) * WSL));
        };
        auto rdwh = [&](int j, int f) {
            const int slot = j % NWS;
            whi[f] = __builtin_amdgcn_ds_read_tr16_b64_v4i16((LAS s16x4*)(LP(wrd[slot >= 3][f]) + (slot % 3) * WSL + 1024));
        };
        BAR();
        ST(2);
        if (PF) touch(lds0, pfa);
#pragma unroll
        for (int mt = 0; mt < 4; ++mt) rdx(0, 0, mt);
#pragma unroll
        for (int f = 0; f < 4; ++f) { rdwl(0, f); rdwh(0, f); }
        SB();
        const u64* pl = fl + FL_LN + (lane & 31);
#pragma unroll
        for (int j = 0; j < 16; ++j) {
            if (j < 15) {
                auto iss = [](int w) { return w < 0 ? 0 : ((w + 5 < 16 ? 2 : 0) + ((w & 1) == 0 && w / 2 + 2 < 8 ? 2 : 0)); };
                int allow;
                if (j == 0) allow = 8 + PF;
                else if (j == 1) allow = 4 + PF + iss(0);
                else if (j == 2) allow = 2 + PF + iss(0) + iss(1);
                else if (j == 3) allow = iss(1) + iss(2);
                else allow = (j & 1) ? iss(j - 2) + iss(j - 1) : iss(j - 3) + iss(j - 2) + iss(j - 1);
                if (j == 14 && wave == 0) wait_vm<1>();
                else switch (allow) {
#define WV(n) case n: wait_vm<n>(); break;
                    WV(0) WV(1) WV(2) WV(3) WV(4) WV(5) WV(6) WV(7) WV(8) WV(9) WV(10) WV(11) WV(12)
#undef WV
                    default: wait_vm<0>(); break;
                }
                BAR();
                SB();
            }
            if (j == 13) { if (wave == 0) poll_issue(pl); SB(); }
            const int cur = j & 1, nxt = cur ^ 1;
            const bool rd = j < 15, lw = j + 5 < 16, lx = (j & 1) == 0 && j / 2 + 2 < 8;
#pragma unroll
            for (int f = 0; f < 4; ++f) {
                const s16x8 wf = __builtin_shufflevector(wlo[f], whi[f], 0, 1, 2, 3, 4, 5, 6, 7);
                acc[f][0] = mfma16(wf, xf[cur][0], acc[f][0]);
                if (rd) rdx(nxt, j + 1, f);
                SB();
                acc[f][1] = mfma16(wf, xf[cur][1], acc[f][1]);
                if (f == 0 && lx) m0x(j / 2 + 2);
                if (f == 1 && lx) LDX(j / 2 + 2, 0);
                if (f == 2 && lw) m0w(j + 5);
                if (f == 3 && lw) LDW(j + 5, 0);
                if (f < 2) csa[f] = mfma16(wf, ones, csa[f]);
                SB();
                acc[f][2] = mfma16(wf, xf[cur][2], acc[f][2]);
                if (rd) rdwl(j + 1, f);
                SB();
                acc[f][3] = mfma16(wf, xf[cur][3], acc[f][3]);
                if (rd) rdwh(j + 1, f);
                if (f == 1 && lx) LDX(j / 2 + 2, 1);
                if (f == 3 && lw) LDW(j + 5, 1);
                SB();
            }
        }
#undef LDX
#undef LDW
#undef LDX2
#undef LDW2
        ST(3);
        if (wave == 0) poll_check<8>(pl, tag DBG_PASS(1));
        if (tid < 8) *(unsigned*)(smem + CTL_OFF + tid * 4) = 0;
        if (m == 0) { f32x4* c = (f32x4*)(smem + CS_OFF) + (wn * 4 + 2 * wm) * 4 + g; c[0] = csa[0]; c[4] = csa[1]; }
        LBAR();
        u32x2 stt[4];
        f32x4 csv[4];
#pragma unroll
        for (int mt = 0; mt < 4; ++mt) stt[mt] = ld_pub8(rs, WS_STATS + (rb * 128 + wm * 64 + 16 * mt + m) * 8);
#pragma unroll
        for (int f = 0; f < 4; ++f) csv[f] = *(const f32x4*)(smem + CS_OFF + ((wn * 4 + f) * 16 + 4 * g) * 4);
        cin.u = ld_pub16(rs, WS_XNP + (dp * SL + tid * 4) * 4);
        cin.ds = A.dskip[dp * 2 + ((tid >> 7) & 1)];
        SB();
        kgen_tables(kv, smem);
        ST(4);
        const int kt = cb * 4 + wn;
        auto epi = [&](auto GS) __attribute__((always_inline)) {
            constexpr int gs = decltype(GS)::value, us = 2 - gs;
#pragma unroll
            for (int mt = 0; mt < 4; ++mt) {
                const float mb = __uint_as_float(stt[mt][0]), rd = __uint_as_float(stt[mt][1]);
                const float nl = rd * -1.44269504088896341f, ird2 = __builtin_amdgcn_rcpf(rd * rd);
                u32x4 o;
#pragma unroll
                for (int u = 0; u < 2; ++u)
#pragma unroll
                    for (int r = 0; r < 2; ++r) {
                        const f32x2 gp = f32x2{acc[gs + u][mt][2 * r], acc[gs + u][mt][2 * r + 1]} - mb * f32x2{csv[u][2 * r], csv[u][2 * r + 1]};
                        const f32x2 up = f32x2{acc[us + u][mt][2 * r], acc[us + u][mt][2 * r + 1]} - mb * f32x2{csv[2 + u][2 * r], csv[2 + u][2 * r + 1]};
                        const f32x2 t = gp * nl;
                        const f32x2 e = {__builtin_amdgcn_exp2f(t[0]), __builtin_amdgcn_exp2f(t[1])};
                        const f32x2 d = __builtin_elementwise_fma(e, f32x2{ird2, ird2}, f32x2{ird2, ird2});
                        const f32x2 h = gp * up * f32x2{__builtin_amdgcn_rcpf(d[0]), __builtin_amdgcn_rcpf(d[1])};
                        o[2 * u + r] = pk2(h[0], h[1]);
                    }
                st_pub16(rs, WS_HF + ((rb * 8 + wm * 4 + mt) * 56 + kt) * 1024 + lane * 16, o);
            }
        };
        if (wm) epi(std::integral_constant<int, 2>{}); else epi(std::integral_constant<int, 0>{});
        if (lane == 0) st_flag(fl + FL_ARR + 8 * b + wave, tag);
        if (wave == 0) poll_issue(fl + FL_WD + min(lane, 27));
        LBAR();
        ST(5);
        conv_uimage(cin, smem);
        conv_kimage(kix, smem);
        if (wave == 0) poll_check<8>(fl + FL_WD + min(lane, 27), tag DBG_PASS(6));
        wait_vm<0>();
        LBAR();
        if (tid == 0) st_flag(fl + FL_H + rb * 16 + cb, tag);
        touch(lds0, A.ws + WS_WDF + (size_t)cb2 * 4 * 57344 + (size_t)((ix & 3) * 448 + min(tid, 447)) * 128);
    } else {
        const int a = (ix - 28) * 8 + xcd;
        AuxX ax;
        kg_issue(dp, A, lds0);
        aux_load_x(a, A, ax);
        SB();
        cin.ds = A.dskip[dp * 2 + ((tid >> 7) & 1)];
        wait_vm<0>();
        const KgIn kin = kg_read(smem, tag);
        if (lane == 0) st_flag(fl + FL_ARR + 8 * b + wave, tag);
        if (tid < 8) *(unsigned*)(smem + CTL_OFF + tid * 4) = 0;
        LBAR();
        ln_part(a, A, ax, smem);
        ST(1);
        if (a < 28) {
            AuxD ad;
            SB();
            aux_load_d(a, A, ad);
            SB();
            wait_vm<8>();
            LBAR();
            if (tid == 0) st_flag(fl + FL_LN + a, tag);
            ST(2);
            wd_part(a, A, ad, smem);
            wait_vm<0>();
            LBAR();
            if (tid == 0) st_flag(fl + FL_WD + a, tag);
        } else {
            wait_vm<0>();
            LBAR();
            if (tid == 0) st_flag(fl + FL_LN + a, tag);
        }
        ST(3);
        conv_kindex(kix);
        kgen_values(kin, kv);
        kgen_tables(kv, smem);
        ST(4);
        if (wave == 0) wait_flags<8>(fl + (lane < 32 ? FL_LN + lane : FL_WD + min(lane - 32, 27)), tag DBG_PASS(2));
        LBAR();
        touch(lds0, A.ws + WS_WDF + (size_t)cb2 * 4 * 57344 + (size_t)((ix & 3) * 448 + min(tid, 447)) * 128);
        cin.u = ld_pub16(rs, WS_XNP + (dp * SL + tid * 4) * 4);
        ST(5);
        conv_uimage(cin, smem);
        conv_kimage(kix, smem);
        LBAR();
    }
    ST(6);
    f32x16 acc0, acc1;
    conv_mma(wave, smem, acc0, acc1);
    conv_handover(wave, smem, acc0, acc1);
    ST(7);
    const int ft = tid >> 3, fc = tid & 7;
    const u32x4 xv = *(const u32x4*)(A.x + (size_t)(rb2 * 64 + ft) * DM + cb2 * 64 + fc * 8);
    if (wave >= 4) {
        const int kw = wave - 4;
        const unsigned hb = WS_HF + ((rb2 * 4) * 56 + kw * 14) * 1024 + lane * 16;
        const unsigned wb = WS_WDF + ((cb2 * 4) * 56 + kw * 14) * 1024 + lane * 16;
        team_wait_flags<2>(wave == 4, lds0 + CTL_OFF + 24, fl + FL_H + (rb2 >> 1) * 16 + min(lane & 15, 13), tag DBG_PASS(3));
        ST(8);
        f32x4 acc[4][4];
#pragma unroll
        for (int i = 0; i < 4; ++i)
#pragma unroll
            for (int j = 0; j < 4; ++j) acc[i][j] = f32x4{0, 0, 0, 0};
        constexpr int PD = G2PD;
        s16x8 Af[PD][4], Bf[PD][4];
        auto lda = [&](int kt, int i) { Af[kt % PD][i] = __builtin_bit_cast(s16x8, ld_pub16(rs, wb + kt * 1024, i * 57344)); };
        auto ldb = [&](int kt, int i) { Bf[kt % PD][i] = __builtin_bit_cast(s16x8, ld_pub16(rs, hb + kt * 1024, i * 57344)); };
#pragma unroll
        for (int kt = 0; kt < PD - 1; ++kt) {
#pragma unroll
            for (int i = 0; i < 4; ++i) { lda(kt, i); ldb(kt, i); }
            SB();
        }
#pragma unroll
        for (int kt = 0; kt < 14; ++kt) {
            const bool ld = kt + PD - 1 < 14;
#pragma unroll
            for (int i = 0; i < 4; ++i) {
#pragma unroll
                for (int j = 0; j < 4; ++j) {
                    acc[i][j] = mfma16(Af[kt % PD][i], Bf[kt % PD][j], acc[i][j]);
                    if (ld && j == 0) lda(kt + PD - 1, i);
                    if (ld && j == 2) ldb(kt + PD - 1, i);
                    SB();
                }
            }
        }
        ST(10);
        const int m = lane & 15, g = lane >> 4;
#pragma unroll
        for (int i = 0; i < 4; ++i)
#pragma unroll
            for (int j = 0; j < 4; ++j) *(f32x4*)(smem + G2_P + ((kw * 4 + i) * 4 + j) * 1024 + (16 * g + (m ^ (4 * i + g))) * 16) = acc[i][j];
    } else {
        conv_finish(wave, dp, A, cin.ds, smem, acc0, acc1);
        ST(8);
        wait_vm<0>();
        unsigned old = 0;
        if (lane == 0) old = __hip_atomic_fetch_add((LAS unsigned*)LP(lds0 + CTL_OFF), 1u, __ATOMIC_RELAXED, __HIP_MEMORY_SCOPE_WORKGROUP);
        if (lane == 0 && old == 3) st_flag(fl + FL_S + dp, tag);
        ST(9);
        if (wave == 0) { __builtin_amdgcn_s_sleep(SWAIT0); }
        team_wait_flags<SWAIT1>(wave == 0, lds0 + CTL_OFF + 28, fl + FL_S + cb2 * 32 + (lane & 31), tag DBG_PASS(4));
        const int t2 = tid;
#pragma unroll
        for (int i = 0; i < 2; ++i) {
            const int d = (t2 >> 3) + 32 * i;
            const u32x4 sv = ld_pub16(rs, WS_SP + ((cb2 * 64 + d) * SL + rb2 * 64 + (t2 & 7) * 8) * 2);
            *(u32x4*)(smem + G2_S + d * 128 + (((t2 & 7) ^ ((d >> 3) & 7)) << 4)) = sv;
        }
        ST(10);
    }
    LBAR();
    ST(11);
    {
        const int mt = ft >> 4, m = ft & 15, dt = fc >> 1, g0 = 2 * (fc & 1);
        f32x4 y0 = {0, 0, 0, 0}, y1 = {0, 0, 0, 0};
#pragma unroll
        for (int s = 0; s < 4; ++s) {
            const char* base = smem + G2_P + ((s * 4 + dt) * 4 + mt) * 1024;
            y0 += *(const f32x4*)(base + (16 * g0 + (m ^ (4 * dt + g0))) * 16);
            y1 += *(const f32x4*)(base + (16 * (g0 + 1) + (m ^ (4 * dt + g0 + 1))) * 16);
        }
        const u16* S = (const u16*)(smem + G2_S + (8 * fc) * 128 + (((ft >> 3) ^ fc) << 4)) + (ft & 7);
        unsigned o[4];
#pragma unroll
        for (int j = 0; j < 4; ++j) {
            const f32x2 f = {rbf(j < 2 ? y0[2 * j] : y1[2 * j - 4]), rbf(j < 2 ? y0[2 * j + 1] : y1[2 * j - 3])};
            const f32x2 sv = {bf2f(S[(2 * j) * 64]), bf2f(S[(2 * j + 1) * 64])};
            const f32x2 gg = {rbf(f[0] * sigm(f[0])), rbf(f[1] * sigm(f[1]))};
            const f32x2 sg = sv * gg;
            o[j] = pk2(bflo(xv[j]) + rbf(sg[0]), bfhi(xv[j]) + rbf(sg[1]));
        }
        *(u32x4*)(A.out + (size_t)(rb2 * 64 + ft) * DM + cb2 * 64 + fc * 8) = u32x4{o[0], o[1], o[2], o[3]};
    }
    ST(12);
    if (b == 0) {
#pragma unroll 1
        for (int i = 0; i < 4; ++i) wait_flags(fl + FL_ARR + (wave * 4 + i) * 64 + lane, tag DBG_PASS(5));
        LBAR();
        if (tid == 0) st_flag(fl + FL_EPOCH, tag);
    }
    ST(15); ST_FLUSH(A.ws, 0);
}

extern "C" void kernel_launch(void* const* d_in, const int* in_sizes, int n_in, void* d_out, int out_size, void* d_ws, size_t ws_size,
                              hipStream_t stream) {
    Args A;
    A.x = (const u16*)d_in[0]; A.Are = (const u16*)d_in[1]; A.Aim = (const u16*)d_in[2]; A.Bre = (const u16*)d_in[3];
    A.Bim = (const u16*)d_in[4]; A.Cre = (const u16*)d_in[5]; A.Cim = (const u16*)d_in[6]; A.logdt = (const u16*)d_in[7];
    A.dskip = (const u16*)d_in[8]; A.wg = (const u16*)d_in[10]; A.wu = (const u16*)d_in[11]; A.wd = (const u16*)d_in[12];
    A.out = (u16*)d_out; A.ws = (char*)d_ws;
    (void)hipFuncSetAttribute((const void*)k_fused, hipFuncAttributeMaxDynamicSharedMemorySize, FUSED_LDS);
    k_fused<<<256, 512, FUSED_LDS, stream>>>(A);
}
```

```cpp
#define FUSED
#define PF 0
#define TLBW 0
#include <hip/hip_runtime.h>
#include <stdint.h>
#include <type_traits>

#define GAS __attribute__((address_space(1)))
#define LAS __attribute__((address_space(3)))
typedef __attribute__((ext_vector_type(4))) short s16x4;
typedef __attribute__((ext_vector_type(8))) short s16x8;
typedef __attribute__((ext_vector_type(8))) __bf16 bf16x8;
typedef __attribute__((ext_vector_type(2))) __bf16 bf16x2;
typedef __attribute__((ext_vector_type(2))) float f32x2;
typedef __attribute__((ext_vector_type(4))) float f32x4;
typedef __attribute__((ext_vector_type(16))) float f32x16;
typedef __attribute__((ext_vector_type(4))) unsigned u32x4;
typedef __attribute__((ext_vector_type(2))) unsigned u32x2;
typedef unsigned short u16;

constexpr int SL = 2048, DM = 512, NS = 128, FF = 1792;

#define SB() __builtin_amdgcn_sched_barrier(0)
#define BAR() do { __builtin_amdgcn_s_barrier(); asm volatile("" ::: "memory"); } while (0)
#define LP(a) ((LAS char*)(size_t)(unsigned)(a))
template <int N> __device__ __forceinline__ void wait_vm() { asm volatile("s_waitcnt vmcnt(%0)" :: "n"(N) : "memory"); }
__device__ __forceinline__ void wait_lgkm0() { asm volatile("s_waitcnt lgkmcnt(0)" ::: "memory"); }
#define LBAR() do { wait_lgkm0(); BAR(); } while (0)

__device__ __forceinline__ float bf2f(u16 v) { return __uint_as_float(((unsigned)v) << 16); }
__device__ __forceinline__ float bflo(unsigned v) { return __uint_as_float(v << 16); }
__device__ __forceinline__ float bfhi(unsigned v) { return __uint_as_float(v & 0xffff0000u); }
__device__ __forceinline__ unsigned pk2(float a, float b) {
    f32x2 z = {a, b};
    return __builtin_bit_cast(unsigned, __builtin_convertvector(z, bf16x2));
}
__device__ __forceinline__ float rbf(float a) { return bflo(pk2(a, 0.f)); }
__device__ __forceinline__ float sigm(float x) { return __builtin_amdgcn_rcpf(1.f + __builtin_amdgcn_exp2f(x * -1.44269504088896341f)); }
__device__ __forceinline__ f32x4 mfma16(s16x8 a, s16x8 b, f32x4 c) {
    return __builtin_amdgcn_mfma_f32_16x16x32_bf16(__builtin_bit_cast(bf16x8, a), __builtin_bit_cast(bf16x8, b), c, 0, 0, 0);
}
__device__ __forceinline__ f32x16 mfma32(s16x8 a, s16x8 b, f32x16 c) {
    return __builtin_amdgcn_mfma_f32_32x32x16_bf16(__builtin_bit_cast(bf16x8, a), __builtin_bit_cast(bf16x8, b), c, 0, 0, 0);
}
__device__ __forceinline__ void set_m0(unsigned lds) { asm volatile("s_mov_b32 m0, %0" :: "s"(lds)); }
template <int I> __device__ __forceinline__ void glds_o(const char* g, unsigned off) {
    asm volatile("global_load_lds_dwordx4 %1, %0 offset:%2" :: "s"(g), "v"(off), "n"(I * 1024));
}

#define ST_DECL
#define ST(i)
#define ST_CLK(i)
#define ST_FLUSH(wsbase, kid)
#define ST_ARG
#define ST_PASS
constexpr size_t PG = 2u << 20;
constexpr size_t WS_FLAG  = 0;
constexpr size_t WS_STATS = 32768;
constexpr size_t WS_XNP   = 1 * PG + 4096;
constexpr size_t WS_HF    = 3 * PG + 4096;
constexpr size_t WS_WDF   = 7 * PG + 4096;
constexpr size_t WS_SP    = 8 * PG + 4096;
constexpr size_t WS_END   = 10 * PG;
__host__ __device__ constexpr unsigned hf_tile(int mt) { return (unsigned)WS_HF + (unsigned)(mt >> 5) * (unsigned)PG + (unsigned)(mt & 31) * 57344u; }
constexpr int FL_EPOCH = 0, FL_LN = 32, FL_CS = 64, FL_WD = 96, FL_H = 256  , FL_S = 512  , FL_ARR = 1024  ;

struct Args {
    const u16 *x, *Are, *Aim, *Bre, *Bim, *Cre, *Cim, *logdt, *dskip, *wg, *wu, *wd;
    u16* out;
    char* ws;
};
typedef __amdgpu_buffer_rsrc_t rsrc_t;
__device__ __forceinline__ rsrc_t ws_rsrc(const Args& A) { return __builtin_amdgcn_make_buffer_rsrc(A.ws, 0, (unsigned)WS_END, 0x00020000); }
constexpr int AUX_PUB = 16;
__device__ __forceinline__ void st_pub16(rsrc_t rs, unsigned off, u32x4 v) { __builtin_amdgcn_raw_buffer_store_b128(v, rs, off, 0, AUX_PUB); }
__device__ __forceinline__ void st_pub8(rsrc_t rs, unsigned off, u32x2 v) { __builtin_amdgcn_raw_buffer_store_b64(v, rs, off, 0, AUX_PUB); }
__device__ __forceinline__ u32x4 ld_pub16(rsrc_t rs, unsigned off, unsigned soff = 0) { return __builtin_amdgcn_raw_buffer_load_b128(rs, off, soff, AUX_PUB); }
__device__ __forceinline__ u32x2 ld_pub8(rsrc_t rs, unsigned off) { return __builtin_amdgcn_raw_buffer_load_b64(rs, off, 0, AUX_PUB); }

template <int CTRL> __device__ __forceinline__ float dpp_f(float x) {
    return __builtin_bit_cast(float, __builtin_amdgcn_mov_dpp(__builtin_bit_cast(int, x), CTRL, 0xf, 0xf, true));
}
__device__ __forceinline__ float row16_sum(float x) {
    x += dpp_f<0x128>(x); x += dpp_f<0x124>(x); x += dpp_f<0x122>(x); x += dpp_f<0x121>(x);
    return x;
}
__device__ __forceinline__ float xrow_sum(float x) {
    auto s = __builtin_amdgcn_permlane16_swap(__float_as_uint(x), __float_as_uint(x), false, false);
    x = __uint_as_float(s[0]) + __uint_as_float(s[1]);
    auto t = __builtin_amdgcn_permlane32_swap(__float_as_uint(x), __float_as_uint(x), false, false);
    return __uint_as_float(t[0]) + __uint_as_float(t[1]);
}
struct AuxX { u32x4 x[8]; };
struct AuxW { u32x4 w[16]; };
struct AuxD { u32x4 d[8]; };
__device__ __forceinline__ void aux_load_x(int a, const Args& A, AuxX& in) {
    const int tid = threadIdx.x, lane = tid & 63, wave = tid >> 6;
#pragma unroll
    for (int i = 0; i < 8; ++i)
        in.x[i] = *(const u32x4*)(A.x + (size_t)(a * 64 + wave * 8 + (i >> 2) * 4 + (lane >> 4)) * DM + ((i & 3) * 16 + (lane & 15)) * 8);
}
__device__ __forceinline__ void aux_load_w(int a, const Args& A, AuxW& in) {
    const int tid = threadIdx.x, c = tid & 7, r = tid >> 3;
#pragma unroll
    for (int i = 0; i < 16; ++i) in.w[i] = *(const u32x4*)(((i >> 3) ? A.wu : A.wg) + (size_t)(r + (i & 7) * 64) * FF + a * 64 + c * 8);
}
__device__ __forceinline__ void aux_load_d(int a, const Args& A, AuxD& in) {
    const int tid = threadIdx.x;
#pragma unroll
    for (int i = 0; i < 8; ++i) { const int cc = i * 512 + tid; in.d[i] = *(const u32x4*)(A.wd + (size_t)(a * 64 + (cc >> 6)) * DM + (cc & 63) * 8); }
}
__device__ __forceinline__ float dot2bf(unsigned a, unsigned b, float c) {
    return __builtin_amdgcn_fdot2_f32_bf16(__builtin_bit_cast(bf16x2, a), __builtin_bit_cast(bf16x2, b), c, false);
}
__device__ __forceinline__ void ln_part(int a, const Args& A, const AuxX& in, char* smem) {
    const int tid = threadIdx.x, lane = tid & 63, wave = tid >> 6;
    const rsrc_t rs = ws_rsrc(A);
#pragma unroll
    for (int ps = 0; ps < 2; ++ps) {
        float s0 = 0.f, s1 = 0.f, q0 = 0.f, q1 = 0.f;
#pragma unroll
        for (int i = 0; i < 4; ++i)
#pragma unroll
            for (int j = 0; j < 4; j += 2) {
                const unsigned u0 = in.x[4 * ps + i][j], u1 = in.x[4 * ps + i][j + 1];
                s0 = dot2bf(u0, 0x3f803f80u, s0); s1 = dot2bf(u1, 0x3f803f80u, s1);
                q0 = dot2bf(u0, u0, q0); q1 = dot2bf(u1, u1, q1);
            }
        const float mean = row16_sum(s0 + s1) * (1.f / DM);
        const float var = rbf(fmaxf(row16_sum(q0 + q1) * (1.f / DM) - mean * mean, 0.f));
        const float den = rbf(__builtin_sqrtf(rbf(var + 1e-5f)));
        const float mb = rbf(mean), rd = 1.f / den, nm = -mb * rd;
        const int r = wave * 8 + ps * 4 + (lane >> 4);
        if ((lane & 15) == 0) st_pub8(rs, WS_STATS + (a * 64 + r) * 8, u32x2{__float_as_uint(mb), __float_as_uint(rd)});
#pragma unroll
        for (int i = 0; i < 4; ++i) {
            u32x4 o;
#pragma unroll
            for (int j = 0; j < 4; ++j) {
                const unsigned u = in.x[4 * ps + i][j];
                const f32x2 v = __builtin_elementwise_fma(f32x2{bflo(u), bfhi(u)}, f32x2{rd, rd}, f32x2{nm, nm});
                o[j] = pk2(v[0], v[1]);
            }
            *(u32x4*)(smem + r * 1024 + (((i * 16 + (lane & 15)) ^ ((r >> 2) & 7)) << 4)) = o;
        }
    }
    LBAR();
    const unsigned* T = (const unsigned*)smem;
#pragma unroll
    for (int it = 0; it < 8; ++it) {
        const int dp = it * 32 + wave * 4 + (lane >> 4), tq = lane & 15;
        u32x4 o;
#pragma unroll
        for (int j = 0; j < 4; ++j) o[j] = T[(4 * tq + j) * 256 + (((dp >> 2) ^ (tq & 7)) << 2) + (dp & 3)];
        st_pub16(rs, WS_XNP + (dp * SL + a * 64 + 4 * tq) * 4, o);
    }
}
__device__ __forceinline__ void wd_part(int a, const Args& A, const AuxD& in, char* smem) {
    const int tid = threadIdx.x;
#pragma unroll
    for (int i = 0; i < 8; ++i) {
        const int c = i * 512 + tid, r = c >> 6, cc = c & 63;
        *(u32x4*)(smem + r * 1024 + ((((cc >> 1) ^ (r & 7)) << 1) | (cc & 1)) * 16) = in.d[i];
    }
    LBAR();
    const rsrc_t rs = ws_rsrc(A);
    const int lane = tid & 63, wave = tid >> 6, g = lane >> 4, q = (lane & 15) >> 2, p = lane & 3;
    const unsigned lds0 = (unsigned)(size_t)(LAS char*)smem;
#pragma unroll
    for (int i = 0; i < 8; ++i) {
        const int f = i * 8 + wave, kk = f >> 5, dt = f & 31;
        const int r0 = 32 * kk + 4 * g + q, r1 = r0 + 16;
        const s16x4 lo = __builtin_amdgcn_ds_read_tr16_b64_v4i16((LAS s16x4*)LP(lds0 + r0 * 1024 + ((dt ^ (r0 & 7)) << 5) + p * 8));
        const s16x4 hi = __builtin_amdgcn_ds_read_tr16_b64_v4i16((LAS s16x4*)LP(lds0 + r1 * 1024 + ((dt ^ (r1 & 7)) << 5) + p * 8));
        st_pub16(rs, WS_WDF + ((dt * 56 + 2 * a + kk) * 64 + lane) * 16, __builtin_bit_cast(u32x4, __builtin_shufflevector(lo, hi, 0, 1, 2, 3, 4, 5, 6, 7)));
    }
}

typedef f32x2 cpx;
__device__ __forceinline__ cpx cmul(cpx a, cpx b) {
    cpx r;
    asm("v_pk_mul_f32 %0, %1, %2 op_sel:[0,0] op_sel_hi:[0,1]\n\t"
        "v_pk_fma_f32 %0, %1, %2, %0 op_sel:[1,1,0] op_sel_hi:[1,0,1] neg_lo:[1,0,0]" : "=&v"(r) : "v"(a), "v"(b));
    return r;
}
__device__ __forceinline__ cpx cexp_p(float are2, float airev, float p) {
    const float er = __builtin_amdgcn_exp2f(are2 * p);
    const float rv = __builtin_amdgcn_fractf(airev * p);
    return cpx{er * __builtin_amdgcn_cosf(rv), er * __builtin_amdgcn_sinf(rv)};
}
struct KgIn { u16 are, aim, bre, bim, cre, cim, ldt; };
__device__ __forceinline__ KgIn kg_load(int dp, const Args& A) {
    const int tid = threadIdx.x, lane = tid & 63, wave = tid >> 6;
    const int d = dp * 2 + ((wave >> 1) & 1), i = d * NS + (wave & 1) * 64 + lane;
    KgIn k;
    k.are = A.Are[i]; k.aim = A.Aim[i]; k.bre = A.Bre[i]; k.bim = A.Bim[i]; k.cre = A.Cre[i]; k.cim = A.Cim[i]; k.ldt = A.logdt[d];
    return k;
}
constexpr int KG_TAB = 16 * 81 * 16;
constexpr int KC_OFF = 88 * 1024;
__device__ __forceinline__ void cmul2(cpx& r0, cpx a0, cpx b0, cpx& r1, cpx a1, cpx b1) {
    asm("v_pk_mul_f32 %0, %2, %3 op_sel:[0,0] op_sel_hi:[0,1]\n\t"
        "v_pk_mul_f32 %1, %4, %5 op_sel:[0,0] op_sel_hi:[0,1]\n\t"
        "v_pk_fma_f32 %0, %2, %3, %0 op_sel:[1,1,0] op_sel_hi:[1,0,1] neg_lo:[1,0,0]\n\t"
        "v_pk_fma_f32 %1, %4, %5, %1 op_sel:[1,1,0] op_sel_hi:[1,0,1] neg_lo:[1,0,0]"
        : "=&v"(r0), "=&v"(r1) : "v"(a0), "v"(b0), "v"(a1), "v"(b1));
}
struct KgVals { unsigned v[41]; };
__device__ __forceinline__ void kgen_values(const KgIn& in, KgVals& o) {
    const int wave = __builtin_amdgcn_readfirstlane(threadIdx.x >> 6);
    const float dt = rbf(expf(bf2f(in.ldt)));
    const float are2 = dt * bf2f(in.are) * 1.44269504088896341f, airev = dt * bf2f(in.aim) * 0.15915494309189535f;
    auto put = [&](int i, cpx v) { o.v[i] = pk2(v[0], v[1]); };
    if (wave < 4) {
        cpx P[16];
        P[0] = cpx{1.f, 0.f};
        P[1] = cexp_p(are2, -airev, 1.f);
        P[2] = cmul(P[1], P[1]);
        cmul2(P[3], P[2], P[1], P[4], P[2], P[2]);
        cmul2(P[5], P[4], P[1], P[6], P[3], P[3]);
        cmul2(P[7], P[6], P[1], P[8], P[4], P[4]);
        cmul2(P[9], P[8], P[1], P[10], P[5], P[5]);
        cmul2(P[11], P[10], P[1], P[12], P[6], P[6]);
        cmul2(P[13], P[12], P[1], P[14], P[7], P[7]);
        P[15] = cmul(P[14], P[1]);
#pragma unroll
        for (int b = 0; b < 16; ++b) put(b, P[b]);
#pragma unroll
        for (int s = 0; s < 3; ++s)
#pragma unroll
            for (int i = 0; i < 8; i += 2) {
                cpx a0 = P[8 + i], a1 = P[9 + i];
                cmul2(P[8 + i], a0, a0, P[9 + i], a1, a1);
                put(16 + 8 * s + i, P[8 + i]); put(17 + 8 * s + i, P[9 + i]);
            }
        o.v[40] = 0;
    } else {
        const cpx dB = {dt * bf2f(in.bre), dt * bf2f(in.bim)};
        const cpx w = cmul(cpx{bf2f(in.cre), bf2f(in.cim)}, dB);
        const cpx z16 = cexp_p(are2, airev, 16.f), z128 = cexp_p(are2, airev, 128.f);
        cpx z32, z64, z256, z512, p0, p1;
        cmul2(z32, z16, z16, z256, z128, z128);
        cmul2(z64, z32, z32, z512, z256, z256);
        const cpx z1024 = cmul(z512, z512);
        p0 = w; p1 = cmul(w, z16);
#pragma unroll
        for (int a = 0; a < 16; a += 2) {
            put(a, p0); put(1 + a, p1);
            if (a < 14) { cpx q0 = p0, q1 = p1; cmul2(p0, q0, z32, p1, q1, z32); }
        }
        cmul2(p0, w, z256, p1, w, z512);
#pragma unroll
        for (int a = 0; a < 8; ++a) {
            put(16 + a, p0); put(24 + a, p1);
            if (a < 7) { cpx q0 = p0, q1 = p1; cmul2(p0, q0, z32, p1, q1, z64); }
        }
        p0 = cmul(w, z1024); p1 = cmul(p0, z128);
#pragma unroll
        for (int a = 0; a < 8; a += 2) {
            put(32 + a, p0); put(33 + a, p1);
            cpx q0 = p0, q1 = p1; cmul2(p0, q0, z256, p1, q1, z256);
        }
        put(40, p0);
    }
#pragma unroll
    for (int i = 0; i < 41; ++i) asm volatile("" : "+v"(o.v[i]));
}
__device__ __forceinline__ void kgen_tables(const KgVals& in, char* smem) {
    const int tid = threadIdx.x, lane = tid & 63, wave = __builtin_amdgcn_readfirstlane(tid >> 6);
    unsigned* tab = (unsigned*)(smem + (wave & 3) * KG_TAB) + (lane >> 2) * (81 * 4) + (lane & 3) + (wave >> 2) * (40 * 4);
#pragma unroll
    for (int i = 0; i < 40; ++i) tab[i * 4] = in.v[i];
    if (wave >= 4) tab[40 * 4] = in.v[40];
    LBAR();
    {
        const int ch = wave >> 2, s = wave & 3, b = lane & 15, g = lane >> 4;
        const int ra = s == 0 ? 40 + b : (s == 3 ? 72 + min(b, 8) : 48 + 8 * s + (b & 7));
        const int sh = min(s, b == 0 ? 3 : __builtin_clz((unsigned)b) - 28), bb = b << sh, e = s - sh;
        const int rb = e > 0 ? 16 + 8 * (e - 1) + (bb - 8) : bb;
        const char* ta = smem + ch * 2 * KG_TAB + (g * 81 + ra) * 16;
        const char* tb = smem + ch * 2 * KG_TAB + (g * 81 + rb) * 16;
        f32x4 acc = {0, 0, 0, 0};
#pragma unroll
        for (int k = 0; k < 8; ++k)
            acc = mfma16(*(const s16x8*)(ta + (k >> 2) * KG_TAB + (k & 3) * (4 * 81 * 16)), *(const s16x8*)(tb + (k >> 2) * KG_TAB + (k & 3) * (4 * 81 * 16)), acc);
        float* kc = (float*)(smem + KC_OFF) + ch * 656 + (s ? 128 * (s + 1) : 0) + 64 * g + b;
        if (s == 0 || g < 2) { kc[0] = acc[0]; kc[16] = acc[1]; kc[32] = acc[2]; kc[48] = acc[3]; }
        else if (s == 3 && g == 2) kc[0] = acc[0];
    }
}

constexpr int KR_C = 2087;
constexpr int CV_KSTR = 4416;
constexpr int CV_KB = 4 * CV_KSTR;
constexpr int CV_UB = 96 * 80;
constexpr int CV_U = 36 * 1024;
constexpr int CV_X = 52 * 1024;
constexpr int G2_S = 52 * 1024;
constexpr int G2_P = 94 * 1024;
constexpr int CTL_OFF = 158 * 1024;
struct CvIn { u32x4 u; u16 ds; };
__device__ __forceinline__ void conv_uimage(const CvIn& in, char* smem) {
    const int tid = threadIdx.x;
    const int idx = 1024 + tid * 4, off = (idx >> 5) * 80 + (idx & 31) * 2;
    *(u32x2*)(smem + CV_U + off) = u32x2{__builtin_amdgcn_perm(in.u[1], in.u[0], 0x05040100), __builtin_amdgcn_perm(in.u[3], in.u[2], 0x05040100)};
    *(u32x2*)(smem + CV_U + CV_UB + off) = u32x2{__builtin_amdgcn_perm(in.u[1], in.u[0], 0x07060302), __builtin_amdgcn_perm(in.u[3], in.u[2], 0x07060302)};
    if (tid < 256) {
        const int ch = tid >> 7, zi = (tid & 127) * 8;
        *(u32x4*)(smem + CV_U + ch * CV_UB + (zi >> 5) * 80 + (zi & 31) * 2) = u32x4{0, 0, 0, 0};
    }
}
struct KIdx { unsigned p[6]; };
__device__ __forceinline__ void conv_kindex(KIdx& o) {
    const int i = threadIdx.x & 255, top = 2050 - 8 * i;
#pragma unroll
    for (int e = 0; e < 12; e += 2) {
        const int l0 = min(top - e, SL - 1), l1 = min(max(top - e - 1, 0), SL - 1);
        const unsigned bb = pk2((float)l0, (float)l1);
        const unsigned i0 = l0 < 128 ? l0 : (bb & 0xffffu) - 17024u, i1 = l1 < 128 ? l1 : (bb >> 16) - 17024u;
        o.p[e >> 1] = (i0 << 2) | (i1 << 18);
        asm volatile("" : "+v"(o.p[e >> 1]));
    }
}
__device__ __forceinline__ void conv_kimage(const KIdx& ix, char* smem) {
    const int tid = threadIdx.x, ch = tid >> 8, i = tid & 255;
    const char* kc = smem + KC_OFF + ch * 656 * 4;
    float v[11];
#pragma unroll
    for (int e = 0; e < 12; e += 2) {
        v[e] = *(const float*)(kc + (ix.p[e >> 1] & 0xffffu));
        if (e + 1 < 11) v[e + 1] = *(const float*)(kc + (ix.p[e >> 1] >> 16));
    }
    if (i == 0) { v[0] = 0.f; v[1] = 0.f; v[2] = 0.f; }
    unsigned ev[5], od[5];
#pragma unroll
    for (int k = 0; k < 5; ++k) { ev[k] = pk2(v[2 * k], v[2 * k + 1]); od[k] = pk2(v[2 * k + 1], v[2 * k + 2]); }
    char* dst = smem + ch * CV_KB + (i + 5) * 16;
    *(u32x4*)(dst) = u32x4{od[1], od[2], od[3], od[4]};
    *(u32x4*)(dst + CV_KSTR) = u32x4{ev[1], ev[2], ev[3], ev[4]};
    *(u32x4*)(dst + 2 * CV_KSTR) = u32x4{od[0], od[1], od[2], od[3]};
    *(u32x4*)(dst + 3 * CV_KSTR) = u32x4{ev[0], ev[1], ev[2], ev[3]};
    if (i == 255) {
        char* e = smem + ch * CV_KB + 261 * 16;
        *(u32x4*)(e) = u32x4{0, 0, 0, 0};
        *(u32x4*)(e + CV_KSTR) = u32x4{pk2(v[10], 0.f), 0, 0, 0};
        *(u32x4*)(e + 2 * CV_KSTR) = u32x4{od[4], 0, 0, 0};
        *(u32x4*)(e + 3 * CV_KSTR) = u32x4{ev[4], pk2(v[10], 0.f), 0, 0};
    }
    if (i < 32) *(u32x4*)(smem + ch * CV_KB + (i >> 3) * CV_KSTR + ((i & 7) < 5 ? (i & 7) : 257 + (i & 7)) * 16) = u32x4{0, 0, 0, 0};
}
template <int Q0, int Q1, int NTILE>
__device__ __forceinline__ void conv_span(unsigned fa, unsigned ua, f32x16& acc0, f32x16& acc1) {
    constexpr int N = Q1 - Q0 + 1, PD = NTILE == 2 ? 4 : 6;
    const unsigned fb = fa - 32 * Q1, ub = ua - ((Q1 + 1) / 2) * 80;
    unsigned fb2 = fb + 8;
    asm volatile("" : "+v"(fb2));
    u32x2 flo[PD], fhi[PD];
    s16x8 b0[PD], b1[PD];
    auto ld = [&](int q, int sl) {
        const int fo = 32 * (Q1 - q);
        flo[sl] = *(const LAS u32x2*)(LP(fb) + fo); fhi[sl] = *(const LAS u32x2*)(LP(fb2) + fo);
        const int qq = q & 1, blk = (q + qq) / 2;
        const int uo = ((Q1 + 1) / 2 - blk) * 80 + 32 * qq;
        if (NTILE == 2) b0[sl] = *(const LAS s16x8*)(LP(ub) + uo);
        b1[sl] = *(const LAS s16x8*)(LP(ub) + uo + 32 * 80);
    };
#pragma unroll
    for (int i = 0; i < PD; ++i) if (i < N) ld(Q0 + i, i);
    SB();
#pragma unroll
    for (int i = 0; i < N; ++i) {
        const int sl = i % PD;
        const s16x8 F = __builtin_bit_cast(s16x8, u32x4{flo[sl][0], flo[sl][1], fhi[sl][0], fhi[sl][1]});
        const s16x8 x0 = b0[sl], x1 = b1[sl];
        if (NTILE == 2) acc0 = mfma32(F, x0, acc0);
        acc1 = mfma32(F, x1, acc1);
        if (i + PD < N) ld(Q0 + i + PD, sl);
        SB();
    }
}
__device__ __forceinline__ void conv_mma(int wave, char* smem, f32x16& acc0, f32x16& acc1) {
    const int lane = threadIdx.x & 63;
    const unsigned lds0 = (unsigned)(size_t)(LAS char*)smem;
    const int ch = (wave >> 1) & 1, part = (wave < 4 ? 2 : 0) + (wave & 1);
    const int r = lane & 31, h = lane >> 5, S = (r + 1) & 3;
    const unsigned fa = lds0 + ch * CV_KB + S * CV_KSTR + (KR_C - r + 8 * h + S) * 2;
    const unsigned ua = lds0 + CV_U + ch * CV_UB + (32 + r) * 80 + 16 * h;
#pragma unroll
    for (int i = 0; i < 16; ++i) { acc0[i] = 0.f; acc1[i] = 0.f; }
    if (part == 0) conv_span<-1, 23, 2>(fa, ua, acc0, acc1);
    else if (part == 1) conv_span<24, 47, 2>(fa, ua, acc0, acc1);
    else if (part == 2) { conv_span<48, 64, 2>(fa, ua, acc0, acc1); conv_span<65, 79, 1>(fa, ua, acc0, acc1); }
    else conv_span<80, 128, 1>(fa, ua, acc0, acc1);
}
__device__ __forceinline__ unsigned lds_ld(unsigned addr) { return __hip_atomic_load((LAS unsigned*)LP(addr), __ATOMIC_RELAXED, __HIP_MEMORY_SCOPE_WORKGROUP); }
__device__ __forceinline__ void lds_st(unsigned addr, unsigned v) { __hip_atomic_store((LAS unsigned*)LP(addr), v, __ATOMIC_RELAXED, __HIP_MEMORY_SCOPE_WORKGROUP); }
__device__ __forceinline__ unsigned lds_add(unsigned addr) { return __hip_atomic_fetch_add((LAS unsigned*)LP(addr), 1u, __ATOMIC_RELAXED, __HIP_MEMORY_SCOPE_WORKGROUP); }
__device__ __forceinline__ void put_tile(char* slot, const f32x16& a) {
    f32x4* X = (f32x4*)slot + (threadIdx.x & 63);
#pragma unroll
    for (int i = 0; i < 4; ++i) X[i * 64] = f32x4{a[4 * i], a[4 * i + 1], a[4 * i + 2], a[4 * i + 3]};
}
__device__ __forceinline__ void add_tile(f32x16& y, const char* slot) {
    const f32x4* X = (const f32x4*)slot + (threadIdx.x & 63);
#pragma unroll
    for (int i = 0; i < 4; ++i) {
        const f32x4 p = X[i * 64];
#pragma unroll
        for (int e = 0; e < 4; ++e) y[4 * i + e] += p[e];
    }
}
__device__ __forceinline__ void conv_handover(int wave, char* smem, const f32x16& acc0, const f32x16& acc1) {
    const unsigned lds0 = (unsigned)(size_t)(LAS char*)smem;
    const int ch = (wave >> 1) & 1;
    char* X = smem + CV_X + ch * 20480;
    if (wave >= 4) { put_tile(X + (wave & 1) * 4096, acc0); put_tile(X + (2 + (wave & 1)) * 4096, acc1); }
    else if ((wave & 1) == 0) put_tile(X + 4 * 4096, acc1);
    if (wave >= 4 || (wave & 1) == 0) {
        wait_lgkm0();
        if ((threadIdx.x & 63) == 0) lds_add(lds0 + CTL_OFF + 8 + ch * 4);
    }
}
__device__ __forceinline__ void conv_finish(int wave, int dp, const Args& A, u16 dsk, char* smem, const f32x16& acc0, const f32x16& acc1) {
    const int lane = threadIdx.x & 63;
    const unsigned lds0 = (unsigned)(size_t)(LAS char*)smem;
    const int ch = (wave >> 1) & 1, o = wave & 1, r = lane & 31, h = lane >> 5;
    while (__builtin_amdgcn_readfirstlane(lds_ld(lds0 + CTL_OFF + 8 + ch * 4)) < 3) __builtin_amdgcn_s_sleep(1);
    asm volatile("" ::: "memory");
    const char* X = smem + CV_X + ch * 20480;
    f32x16 y;
    if (o == 0) { y = acc0; add_tile(y, X); add_tile(y, X + 4096); }
    else { y = acc1; add_tile(y, X + 2 * 4096); add_tile(y, X + 3 * 4096); add_tile(y, X + 4 * 4096); }
    const float ds = bf2f(dsk);
    char* S = smem + CV_X + ch * 20480 + o * 2 * 4096;
    const char* U = smem + CV_U + ch * CV_UB + (32 + 32 * o + r) * 80 + 8 * h;
#pragma unroll
    for (int i = 0; i < 4; ++i) {
        const u32x2 uu = *(const u32x2*)(U + 16 * i);
        const f32x2 y0 = f32x2{y[4 * i], y[4 * i + 1]} + f32x2{bflo(uu[0]), bfhi(uu[0])} * ds;
        const f32x2 y1 = f32x2{y[4 * i + 2], y[4 * i + 3]} + f32x2{bflo(uu[1]), bfhi(uu[1])} * ds;
        *(u32x2*)(S + r * 64 + ((i ^ ((r >> 1) & 3)) << 4) + h * 8) = u32x2{pk2(y0[0], y0[1]), pk2(y1[0], y1[1])};
    }
    wait_lgkm0();
    const rsrc_t rs = ws_rsrc(A);
#pragma unroll
    for (int i = 0; i < 2; ++i)
        st_pub16(rs, WS_SP + ((dp * 2 + ch) * SL + 1024 * o) * 2 + i * 1024 + lane * 16, *(const u32x4*)(S + i * 1024 + (lane >> 2) * 64 + (((lane & 3) ^ ((lane >> 3) & 3)) << 4)));
}
typedef unsigned long long u64;
__device__ __forceinline__ void st_flag(u64* p, u64 v) { __hip_atomic_store((GAS u64*)p, v, __ATOMIC_RELAXED, __HIP_MEMORY_SCOPE_AGENT); }
__device__ __forceinline__ u64 ld_flag(const u64* p) { return __hip_atomic_load((GAS u64*)p, __ATOMIC_RELAXED, __HIP_MEMORY_SCOPE_AGENT); }
#define DBG_TMO(code)
#define DBG_ARG
#define DBG_PASS(c)
template <int SLEEP = 4>
__device__ __forceinline__ void wait_flags(const u64* p, u64 tag DBG_ARG) {
    const u64 t0 = __builtin_amdgcn_s_memrealtime();
    while (!__all(ld_flag(p) == tag)) {
        if (__builtin_amdgcn_s_memrealtime() - t0 > 300000ull) { DBG_TMO(code); break; }
        __builtin_amdgcn_s_sleep(SLEEP);
    }
    asm volatile("" ::: "memory");
}
template <int SLEEP = 4>
__device__ __forceinline__ void team_wait_flags(bool poller, unsigned lds_word, const u64* p, u64 tag DBG_ARG) {
    if (poller) {
        wait_flags<SLEEP>(p, tag);
        if ((threadIdx.x & 63) == 0) lds_st(lds_word, 1u);
    } else {
        while (__builtin_amdgcn_readfirstlane(lds_ld(lds_word)) == 0) __builtin_amdgcn_s_sleep(4);
        asm volatile("" ::: "memory");
    }
}
__device__ __forceinline__ void poll_issue(const u64* p) { asm volatile("global_load_dwordx2 v[254:255], %0, off sc1" :: "v"(p) : "v254", "v255"); }
template <int SLEEP = 4>
__device__ __forceinline__ void poll_check(const u64* p, u64 tag DBG_ARG) {
    unsigned lo, hi;
    asm volatile("s_waitcnt vmcnt(0)\n\tv_mov_b32 %0, v254\n\tv_mov_b32 %1, v255" : "=v"(lo), "=v"(hi) :: "memory");
    const u64 v = ((u64)hi << 32) | lo;
    if (!__all(v == tag)) wait_flags<SLEEP>(p, tag);
}
template <int OFF> __device__ __forceinline__ void glds_dw(const void* p) {
    asm volatile("global_load_lds_dword %0, off offset:%1" :: "v"((const char*)p - OFF), "n"(OFF));
}
template <int OFF> __device__ __forceinline__ void glds_dw_sc1(const void* p) {
    asm volatile("global_load_lds_dword %0, off offset:%1 sc1" :: "v"((const char*)p - OFF), "n"(OFF));
}
constexpr int DUMMY_OFF = CTL_OFF + 512;
__device__ __forceinline__ void touch(unsigned lds0, const void* p) { set_m0(lds0 + DUMMY_OFF); asm volatile("s_nop 0"); glds_dw_sc1<0>(p); }
__device__ __forceinline__ unsigned kg_area(int wave) { return 32 * 1024 + wave * 2048; }
__device__ __forceinline__ void kg_issue(int dp, const Args& A, unsigned lds0) {
    const int tid = threadIdx.x, lane = tid & 63, wave = __builtin_amdgcn_readfirstlane(tid >> 6);
    const int d = dp * 2 + ((wave >> 1) & 1), i = d * NS + 2 * lane;
    set_m0(lds0 + kg_area(wave));
    asm volatile("s_nop 0");
    glds_dw<0>(A.Are + i); glds_dw<256>(A.Aim + i); glds_dw<512>(A.Bre + i); glds_dw<768>(A.Bim + i);
    glds_dw<1024>(A.Cre + i); glds_dw<1280>(A.Cim + i); glds_dw<1536>(A.logdt + (d & ~1));
    glds_dw_sc1<7 * 256>((const char*)(A.ws + WS_FLAG) + FL_EPOCH * 8 + (lane & 1) * 4);
}
__device__ __forceinline__ KgIn kg_read(const char* smem, u64& tag) {
    const int tid = threadIdx.x, lane = tid & 63, wave = __builtin_amdgcn_readfirstlane(tid >> 6);
    const char* ar = smem + kg_area(wave);
    const u16* pm = (const u16*)ar + (wave & 1) * 64 + lane;
    KgIn r;
    r.are = pm[0]; r.aim = pm[128]; r.bre = pm[256]; r.bim = pm[384]; r.cre = pm[512]; r.cim = pm[640];
    r.ldt = ((const u16*)(ar + 1536))[(wave >> 1) & 1];
    tag = *(const u64*)(ar + 1792) + 1;
    wait_lgkm0();
    return r;
}

constexpr int XS = 16384, WSL = 16384, NXS = 3, NWS = 6;
constexpr int CS_OFF = CTL_OFF + 1024;
constexpr int FUSED_LDS = 160 * 1024;
#define SWAIT0 48
#define SWAIT1 24
#define CVPRIO 2
#define CSM 1
#define NO_CS 0
#define G2PD 5
#define TLBW_CODE_PAGES 6
#define TLBW_BACK 0

__device__ __forceinline__ void tlb_warm(const Args& A, unsigned lds0) {
    const int lane = threadIdx.x & 63;
    unsigned long long pc;
    asm volatile("s_getpc_b64 %0" : "=s"(pc));
    const char* pa;
    if (lane < 16) pa = (const char*)(pc & ~4095ull) + min(lane, TLBW_CODE_PAGES) * 4096 - TLBW_BACK;
    else if (lane < 26) { const int pg = max(lane - 16, 1); pa = A.ws + (size_t)pg * PG + (pg == 2 || pg == 9 ? 8192 : 3968); }
    else if (lane < 28) pa = (const char*)A.out + (lane - 26) * (1u << 20);
    else if (lane == 28) pa = (const char*)A.dskip;
    else pa = (const char*)A.wd + min(lane - 29, 1) * (1u << 20);
    set_m0(lds0 + DUMMY_OFF);
    asm volatile("s_nop 0");
    glds_dw<0>(pa);
}

__global__ __launch_bounds__(512, 2) void k_fused(Args A) {
    extern __shared__ __attribute__((aligned(16))) char smem[];
    ST_DECL; ST(0);
    const int tid = threadIdx.x, lane = tid & 63, wave = __builtin_amdgcn_readfirstlane(tid >> 6);
    const int b = blockIdx.x, xcd = b & 7, ix = b >> 3;
    const int dp = b;
    const int rb2 = xcd * 4 + (ix & 3), cb2 = ix >> 2;
    u64* fl = (u64*)(A.ws + WS_FLAG);
    const rsrc_t rs = ws_rsrc(A);
    const unsigned lds0 = (unsigned)(size_t)(LAS char*)smem;
    u64 tag;
    CvIn cin;

    KgVals kv;
    KIdx kix;
    if (ix < 28) {
        const int rb = (xcd & 3) * 4 + (ix & 3), cb = (xcd >> 2) * 7 + (ix >> 2);
        const int wm = wave & 1, wn = wave >> 1;
        unsigned xoff[2], woff[2];
#pragma unroll
        for (int i = 0; i < 2; ++i) {
            const int ci = (wave * 2 + i) * 64 + lane, row = ci >> 3, pos = ci & 7, ch = pos ^ ((row >> 1) & 7);
            xoff[i] = ((rb * 128 + row) * DM + ch * 8) * 2 - i * 1024 + 4096;
        }
#pragma unroll
        for (int i = 0; i < 2; ++i) {
            const int ci = ((wave & 3) * 2 + i) * 64 + lane, k = ci >> 4, pos = ci & 15;
            const int f = (k & 3) | (((k >> 3) & 1) << 2);
            const int c = (((pos >> 1) ^ f) << 1) | (pos & 1);
            woff[i] = (k * FF + cb * 128 + c * 8) * 2 - i * 1024 + 4096;
        }
        const char* xb = (const char*)A.x - 4096;
        const char* wb = (const char*)((wave >> 2) ? A.wu : A.wg) - 4096;
        const unsigned xdst = lds0 + wave * 2048, wdst = lds0 + NXS * XS + wave * 2048;
        auto m0x = [&](int X) { set_m0(xdst + (X % NXS) * XS); };
        auto m0w = [&](int j) { set_m0(wdst + (j % NWS) * WSL); };
#define LDX(X, I) glds_o<I>(xb + (X) * 128, xoff[I])
#define LDW(J, I) glds_o<I>(wb + (size_t)(J) * (32 * FF * 2), woff[I])
#define LDX2(X) do { m0x(X); asm volatile("s_nop 0"); LDX(X, 0); LDX(X, 1); } while (0)
#define LDW2(J) do { m0w(J); asm volatile("s_nop 0"); LDW(J, 0); LDW(J, 1); } while (0)
        LDX2(0); LDW2(0);
        kg_issue(dp, A, lds0);
        LDW2(1); LDX2(1); LDW2(2); LDW2(3); LDW2(4);
        if (TLBW && wave == 7) tlb_warm(A, lds0);
        SB();
        conv_kindex(kix);
        const int m = lane & 15, g = lane >> 4, q = (lane & 15) >> 2, p = lane & 3;
        const int fx = (m >> 1) & 7;
        int xrd[2];
#pragma unroll
        for (int ks = 0; ks < 2; ++ks) xrd[ks] = lds0 + (wm * 64 + m) * 128 + (((4 * ks + g) ^ fx) << 4);
        const int fw = q | ((g & 1) << 2);
        int wrd[2][4];
#pragma unroll
        for (int f = 0; f < 4; ++f) {
            wrd[0][f] = lds0 + NXS * XS + (8 * g + q) * 256 + (((wn * 2 + (f & 1)) ^ fw) << 5) + p * 8 + ((f >> 1) ^ wm) * 8192;
            wrd[1][f] = wrd[0][f] + 3 * WSL;
        }
        const char* pfa;
        if (tid < 352) { const int idx = (ix & 3) * 352 + tid, mat = idx >= 704, rem = idx - 704 * mat;
            pfa = (const char*)(mat ? A.wu : A.wg) + (size_t)(160 + (rem >> 1)) * (FF * 2) + cb * 256 + (rem & 1) * 128; }
        else { const int idx = min((ix >> 2) * 110 + min(tid - 352, 109), 767), row = idx / 6;
            pfa = (const char*)A.x + (size_t)(rb * 128 + row) * 1024 + (2 + idx - 6 * row) * 128; }
        if (TLBW && wave == 7) wait_vm<11>(); else wait_vm<10>();
        {
            const KgIn kin = kg_read(smem, tag);
            ST(1);
            kgen_values(kin, kv);
        }
        f32x4 acc[4][4];
#pragma unroll
        for (int i = 0; i < 4; ++i)
#pragma unroll
            for (int j = 0; j < 4; ++j) acc[i][j] = f32x4{0, 0, 0, 0};
        f32x4 csa[2] = {f32x4{0, 0, 0, 0}, f32x4{0, 0, 0, 0}};
        const s16x8 ones = {0x3f80, 0x3f80, 0x3f80, 0x3f80, 0x3f80, 0x3f80, 0x3f80, 0x3f80};
        s16x8 xf[2][4];
        s16x4 wlo[4], whi[4];
        auto rdx = [&](int buf, int j, int mt) { xf[buf][mt] = *(const LAS s16x8*)(LP(xrd[j & 1]) + ((j >> 1) % NXS) * XS + mt * 2048); };
        auto rdwl = [&](int j, int f) {
            const int slot = j % NWS;
            wlo[f] = __builtin_amdgcn_ds_read_tr16_b64_v4i16((LAS s16x4*)(LP(wrd[slot >= 3][f]) + (slot % 3) * WSL));
        };
        auto rdwh = [&](int j, int f) {
            const int slot = j % NWS;
            whi[f] = __builtin_amdgcn_ds_read_tr16_b64_v4i16((LAS s16x4*)(LP(wrd[slot >= 3][f]) + (slot % 3) * WSL + 1024));
        };
        BAR();
        ST(2);
        if (PF) touch(lds0, pfa);
#pragma unroll
        for (int mt = 0; mt < 4; ++mt) rdx(0, 0, mt);
#pragma unroll
        for (int f = 0; f < 4; ++f) { rdwl(0, f); rdwh(0, f); }
        SB();
        const u64* pl = fl + FL_LN + (lane & 31);
#pragma unroll
        for (int j = 0; j < 16; ++j) {
            if (j < 15) {
                auto iss = [](int w) { return w < 0 ? 0 : ((w + 5 < 16 ? 2 : 0) + ((w & 1) == 0 && w / 2 + 2 < 8 ? 2 : 0)); };
                int allow;
                if (j == 0) allow = 8 + PF;
                else if (j == 1) allow = 4 + PF + iss(0);
                else if (j == 2) allow = 2 + PF + iss(0) + iss(1);
                else if (j == 3) allow = iss(1) + iss(2);
                else allow = (j & 1) ? iss(j - 2) + iss(j - 1) : iss(j - 3) + iss(j - 2) + iss(j - 1);
                if (j == 14 && wave == 0) wait_vm<1>();
                else if (TLBW && j <= 2 && wave == 7) {
                    if (allow + 1 == 9) wait_vm<9>(); else if (allow + 1 == 10) wait_vm<10>(); else if (allow + 1 == 11) wait_vm<11>(); else wait_vm<0>();
                }
                else switch (allow) {
#define WV(n) case n: wait_vm<n>(); break;
                    WV(0) WV(1) WV(2) WV(3) WV(4) WV(5) WV(6) WV(7) WV(8) WV(9) WV(10) WV(11) WV(12)
#undef WV
                    default: wait_vm<0>(); break;
                }
                BAR();
                SB();
            }
            if (j == 13) { if (wave == 0) poll_issue(pl); SB(); }
            const int cur = j & 1, nxt = cur ^ 1;
            const bool rd = j < 15, lw = j + 5 < 16, lx = (j & 1) == 0 && j / 2 + 2 < 8;
#pragma unroll
            for (int f = 0; f < 4; ++f) {
                const s16x8 wf = __builtin_shufflevector(wlo[f], whi[f], 0, 1, 2, 3, 4, 5, 6, 7);
                acc[f][0] = mfma16(wf, xf[cur][0], acc[f][0]);
                if (rd) rdx(nxt, j + 1, f);
                SB();
                acc[f][1] = mfma16(wf, xf[cur][1], acc[f][1]);
                if (f == 0 && lx) m0x(j / 2 + 2);
                if (f == 1 && lx) LDX(j / 2 + 2, 0);
                if (f == 2 && lw) m0w(j + 5);
                if (f == 3 && lw) LDW(j + 5, 0);
                if (f < 2) csa[f] = mfma16(wf, ones, csa[f]);
                SB();
                acc[f][2] = mfma16(wf, xf[cur][2], acc[f][2]);
                if (rd) rdwl(j + 1, f);
                SB();
                acc[f][3] = mfma16(wf, xf[cur][3], acc[f][3]);
                if (rd) rdwh(j + 1, f);
                if (f == 1 && lx) LDX(j / 2 + 2, 1);
                if (f == 3 && lw) LDW(j + 5, 1);
                SB();
            }
        }
#undef LDX
#undef LDW
#undef LDX2
#undef LDW2
        ST(3);
        if (wave == 0) poll_check<8>(pl, tag DBG_PASS(1));
        if (tid < 8) *(unsigned*)(smem + CTL_OFF + tid * 4) = 0;
        if (m == 0) { f32x4* c = (f32x4*)(smem + CS_OFF) + (wn * 4 + 2 * wm) * 4 + g; c[0] = csa[0]; c[4] = csa[1]; }
        LBAR();
        u32x2 stt[4];
        f32x4 csv[4];
#pragma unroll
        for (int mt = 0; mt < 4; ++mt) stt[mt] = ld_pub8(rs, WS_STATS + (rb * 128 + wm * 64 + 16 * mt + m) * 8);
#pragma unroll
        for (int f = 0; f < 4; ++f) csv[f] = *(const f32x4*)(smem + CS_OFF + ((wn * 4 + f) * 16 + 4 * g) * 4);
        cin.u = ld_pub16(rs, WS_XNP + (dp * SL + tid * 4) * 4);
        cin.ds = A.dskip[dp * 2 + ((tid >> 7) & 1)];
        SB();
        kgen_tables(kv, smem);
        ST(4);
        const int kt = cb * 4 + wn;
        auto epi = [&](auto GS) __attribute__((always_inline)) {
            constexpr int gs = decltype(GS)::value, us = 2 - gs;
#pragma unroll
            for (int mt = 0; mt < 4; ++mt) {
                const float mb = __uint_as_float(stt[mt][0]), rd = __uint_as_float(stt[mt][1]);
                const float nl = rd * -1.44269504088896341f, ird2 = __builtin_amdgcn_rcpf(rd * rd);
                u32x4 o;
#pragma unroll
                for (int u = 0; u < 2; ++u)
#pragma unroll
                    for (int r = 0; r < 2; ++r) {
                        const f32x2 gp = f32x2{acc[gs + u][mt][2 * r], acc[gs + u][mt][2 * r + 1]} - mb * f32x2{csv[u][2 * r], csv[u][2 * r + 1]};
                        const f32x2 up = f32x2{acc[us + u][mt][2 * r], acc[us + u][mt][2 * r + 1]} - mb * f32x2{csv[2 + u][2 * r], csv[2 + u][2 * r + 1]};
                        const f32x2 t = gp * nl;
                        const f32x2 e = {__builtin_amdgcn_exp2f(t[0]), __builtin_amdgcn_exp2f(t[1])};
                        const f32x2 d = __builtin_elementwise_fma(e, f32x2{ird2, ird2}, f32x2{ird2, ird2});
                        const f32x2 h = gp * up * f32x2{__builtin_amdgcn_rcpf(d[0]), __builtin_amdgcn_rcpf(d[1])};
                        o[2 * u + r] = pk2(h[0], h[1]);
                    }
                st_pub16(rs, hf_tile(rb * 8 + wm * 4 + mt) + kt * 1024 + lane * 16, o);
            }
        };
        if (wm) epi(std::integral_constant<int, 2>{}); else epi(std::integral_constant<int, 0>{});
        if (lane == 0) st_flag(fl + FL_ARR + 8 * b + wave, tag);
        if (wave == 0) poll_issue(fl + FL_WD + min(lane, 27));
        LBAR();
        ST(5);
        conv_uimage(cin, smem);
        conv_kimage(kix, smem);
        if (wave == 0) poll_check<8>(fl + FL_WD + min(lane, 27), tag DBG_PASS(6));
        wait_vm<0>();
        LBAR();
        if (tid == 0) st_flag(fl + FL_H + rb * 16 + cb, tag);
        touch(lds0, A.ws + WS_WDF + (size_t)cb2 * 4 * 57344 + (size_t)((ix & 3) * 448 + min(tid, 447)) * 128);
    } else {
        const int a = (ix - 28) * 8 + xcd;
        AuxX ax;
        kg_issue(dp, A, lds0);
        aux_load_x(a, A, ax);
        SB();
        cin.ds = A.dskip[dp * 2 + ((tid >> 7) & 1)];
        wait_vm<0>();
        const KgIn kin = kg_read(smem, tag);
        if (lane == 0) st_flag(fl + FL_ARR + 8 * b + wave, tag);
        if (tid < 8) *(unsigned*)(smem + CTL_OFF + tid * 4) = 0;
        LBAR();
        ln_part(a, A, ax, smem);
        ST(1);
        if (a < 28) {
            AuxD ad;
            SB();
            aux_load_d(a, A, ad);
            SB();
            wait_vm<8>();
            LBAR();
            if (tid == 0) st_flag(fl + FL_LN + a, tag);
            ST(2);
            wd_part(a, A, ad, smem);
            wait_vm<0>();
            LBAR();
            if (tid == 0) st_flag(fl + FL_WD + a, tag);
        } else {
            wait_vm<0>();
            LBAR();
            if (tid == 0) st_flag(fl + FL_LN + a, tag);
        }
        ST(3);
        conv_kindex(kix);
        kgen_values(kin, kv);
        kgen_tables(kv, smem);
        ST(4);
        if (wave == 0) wait_flags<8>(fl + (lane < 32 ? FL_LN + lane : FL_WD + min(lane - 32, 27)), tag DBG_PASS(2));
        LBAR();
        touch(lds0, A.ws + WS_WDF + (size_t)cb2 * 4 * 57344 + (size_t)((ix & 3) * 448 + min(tid, 447)) * 128);
        cin.u = ld_pub16(rs, WS_XNP + (dp * SL + tid * 4) * 4);
        ST(5);
        conv_uimage(cin, smem);
        conv_kimage(kix, smem);
        LBAR();
    }
    ST(6);
    f32x16 acc0, acc1;
    conv_mma(wave, smem, acc0, acc1);
    conv_handover(wave, smem, acc0, acc1);
    ST(7);
    const int ft = tid >> 3, fc = tid & 7;
    const u32x4 xv = *(const u32x4*)(A.x + (size_t)(rb2 * 64 + ft) * DM + cb2 * 64 + fc * 8);
    if (wave >= 4) {
        const int kw = wave - 4;
        const unsigned hb = hf_tile(rb2 * 4) + kw * 14 * 1024 + lane * 16;
        const unsigned wb = WS_WDF + ((cb2 * 4) * 56 + kw * 14) * 1024 + lane * 16;
        team_wait_flags<2>(wave == 4, lds0 + CTL_OFF + 24, fl + FL_H + (rb2 >> 1) * 16 + min(lane & 15, 13), tag DBG_PASS(3));
        ST(8);
        f32x4 acc[4][4];
#pragma unroll
        for (int i = 0; i < 4; ++i)
#pragma unroll
            for (int j = 0; j < 4; ++j) acc[i][j] = f32x4{0, 0, 0, 0};
        constexpr int PD = G2PD;
        s16x8 Af[PD][4], Bf[PD][4];
        auto lda = [&](int kt, int i) { Af[kt % PD][i] = __builtin_bit_cast(s16x8, ld_pub16(rs, wb + kt * 1024, i * 57344)); };
        auto ldb = [&](int kt, int i) { Bf[kt % PD][i] = __builtin_bit_cast(s16x8, ld_pub16(rs, hb + kt * 1024, i * 57344)); };
#pragma unroll
        for (int kt = 0; kt < PD - 1; ++kt) {
#pragma unroll
            for (int i = 0; i < 4; ++i) { lda(kt, i); ldb(kt, i); }
            SB();
        }
#pragma unroll
        for (int kt = 0; kt < 14; ++kt) {
            const bool ld = kt + PD - 1 < 14;
#pragma unroll
            for (int i = 0; i < 4; ++i) {
#pragma unroll
                for (int j = 0; j < 4; ++j) {
                    acc[i][j] = mfma16(Af[kt % PD][i], Bf[kt % PD][j], acc[i][j]);
                    if (ld && j == 0) lda(kt + PD - 1, i);
                    if (ld && j == 2) ldb(kt + PD - 1, i);
                    SB();
                }
            }
        }
        ST(10);
        const int m = lane & 15, g = lane >> 4;
#pragma unroll
        for (int i = 0; i < 4; ++i)
#pragma unroll
            for (int j = 0; j < 4; ++j) *(f32x4*)(smem + G2_P + ((kw * 4 + i) * 4 + j) * 1024 + (16 * g + (m ^ (4 * i + g))) * 16) = acc[i][j];
    } else {
        conv_finish(wave, dp, A, cin.ds, smem, acc0, acc1);
        ST(8);
        wait_vm<0>();
        unsigned old = 0;
        if (lane == 0) old = __hip_atomic_fetch_add((LAS unsigned*)LP(lds0 + CTL_OFF), 1u, __ATOMIC_RELAXED, __HIP_MEMORY_SCOPE_WORKGROUP);
        if (lane == 0 && old == 3) st_flag(fl + FL_S + dp, tag);
        ST(9);
        if (wave == 0) { __builtin_amdgcn_s_sleep(SWAIT0); }
        team_wait_flags<SWAIT1>(wave == 0, lds0 + CTL_OFF + 28, fl + FL_S + cb2 * 32 + (lane & 31), tag DBG_PASS(4));
        const int t2 = tid;
#pragma unroll
        for (int i = 0; i < 2; ++i) {
            const int d = (t2 >> 3) + 32 * i;
            const u32x4 sv = ld_pub16(rs, WS_SP + ((cb2 * 64 + d) * SL + rb2 * 64 + (t2 & 7) * 8) * 2);
            *(u32x4*)(smem + G2_S + d * 128 + (((t2 & 7) ^ ((d >> 3) & 7)) << 4)) = sv;
        }
        ST(10);
    }
    LBAR();
    ST(11);
    {
        const int mt = ft >> 4, m = ft & 15, dt = fc >> 1, g0 = 2 * (fc & 1);
        f32x4 y0 = {0, 0, 0, 0}, y1 = {0, 0, 0, 0};
#pragma unroll
        for (int s = 0; s < 4; ++s) {
            const char* base = smem + G2_P + ((s * 4 + dt) * 4 + mt) * 1024;
            y0 += *(const f32x4*)(base + (16 * g0 + (m ^ (4 * dt + g0))) * 16);
            y1 += *(const f32x4*)(base + (16 * (g0 + 1) + (m ^ (4 * dt + g0 + 1))) * 16);
        }
        const u16* S = (const u16*)(smem + G2_S + (8 * fc) * 128 + (((ft >> 3) ^ fc) << 4)) + (ft & 7);
        unsigned o[4];
#pragma unroll
        for (int j = 0; j < 4; ++j) {
            const f32x2 f = {rbf(j < 2 ? y0[2 * j] : y1[2 * j - 4]), rbf(j < 2 ? y0[2 * j + 1] : y1[2 * j - 3])};
            const f32x2 sv = {bf2f(S[(2 * j) * 64]), bf2f(S[(2 * j + 1) * 64])};
            const f32x2 gg = {rbf(f[0] * sigm(f[0])), rbf(f[1] * sigm(f[1]))};
            const f32x2 sg = sv * gg;
            o[j] = pk2(bflo(xv[j]) + rbf(sg[0]), bfhi(xv[j]) + rbf(sg[1]));
        }
        *(u32x4*)(A.out + (size_t)(rb2 * 64 + ft) * DM + cb2 * 64 + fc * 8) = u32x4{o[0], o[1], o[2], o[3]};
    }
    ST(12);
    if (b == 0) {
#pragma unroll 1
        for (int i = 0; i < 4; ++i) wait_flags(fl + FL_ARR + (wave * 4 + i) * 64 + lane, tag DBG_PASS(5));
        LBAR();
        if (tid == 0) st_flag(fl + FL_EPOCH, tag);
    }
    ST(15); ST_FLUSH(A.ws, 0);
}

extern "C" void kernel_launch(void* const* d_in, const int* in_sizes, int n_in, void* d_out, int out_size, void* d_ws, size_t ws_size,
                              hipStream_t stream) {
    Args A;
    A.x = (const u16*)d_in[0]; A.Are = (const u16*)d_in[1]; A.Aim = (const u16*)d_in[2]; A.Bre = (const u16*)d_in[3];
    A.Bim = (const u16*)d_in[4]; A.Cre = (const u16*)d_in[5]; A.Cim = (const u16*)d_in[6]; A.logdt = (const u16*)d_in[7];
    A.dskip = (const u16*)d_in[8]; A.wg = (const u16*)d_in[10]; A.wu = (const u16*)d_in[11]; A.wd = (const u16*)d_in[12];
    A.out = (u16*)d_out; A.ws = (char*)d_ws;
    (void)hipFuncSetAttribute((const void*)k_fused, hipFuncAttributeMaxDynamicSharedMemorySize, FUSED_LDS);
    k_fused<<<256, 512, FUSED_LDS, stream>>>(A);
}
```

```cpp
#define FUSED
#define PF 0
#define DIAG 15
#define DIAG_K 0
#include <hip/hip_runtime.h>
#include <stdint.h>
#include <type_traits>

#define GAS __attribute__((address_space(1)))
#define LAS __attribute__((address_space(3)))
typedef __attribute__((ext_vector_type(4))) short s16x4;
typedef __attribute__((ext_vector_type(8))) short s16x8;
typedef __attribute__((ext_vector_type(8))) __bf16 bf16x8;
typedef __attribute__((ext_vector_type(2))) __bf16 bf16x2;
typedef __attribute__((ext_vector_type(2))) float f32x2;
typedef __attribute__((ext_vector_type(4))) float f32x4;
typedef __attribute__((ext_vector_type(16))) float f32x16;
typedef __attribute__((ext_vector_type(4))) unsigned u32x4;
typedef __attribute__((ext_vector_type(2))) unsigned u32x2;
typedef unsigned short u16;

constexpr int SL = 2048, DM = 512, NS = 128, FF = 1792;

#define SB() __builtin_amdgcn_sched_barrier(0)
#define BAR() do { __builtin_amdgcn_s_barrier(); asm volatile("" ::: "memory"); } while (0)
#define LP(a) ((LAS char*)(size_t)(unsigned)(a))
template <int N> __device__ __forceinline__ void wait_vm() { asm volatile("s_waitcnt vmcnt(%0)" :: "n"(N) : "memory"); }
__device__ __forceinline__ void wait_lgkm0() { asm volatile("s_waitcnt lgkmcnt(0)" ::: "memory"); }
#define LBAR() do { wait_lgkm0(); BAR(); } while (0)

__device__ __forceinline__ float bf2f(u16 v) { return __uint_as_float(((unsigned)v) << 16); }
__device__ __forceinline__ float bflo(unsigned v) { return __uint_as_float(v << 16); }
__device__ __forceinline__ float bfhi(unsigned v) { return __uint_as_float(v & 0xffff0000u); }
__device__ __forceinline__ unsigned pk2(float a, float b) {
    f32x2 z = {a, b};
    return __builtin_bit_cast(unsigned, __builtin_convertvector(z, bf16x2));
}
__device__ __forceinline__ float rbf(float a) { return bflo(pk2(a, 0.f)); }
__device__ __forceinline__ float sigm(float x) { return __builtin_amdgcn_rcpf(1.f + __builtin_amdgcn_exp2f(x * -1.44269504088896341f)); }
__device__ __forceinline__ f32x4 mfma16(s16x8 a, s16x8 b, f32x4 c) {
    return __builtin_amdgcn_mfma_f32_16x16x32_bf16(__builtin_bit_cast(bf16x8, a), __builtin_bit_cast(bf16x8, b), c, 0, 0, 0);
}
__device__ __forceinline__ f32x16 mfma32(s16x8 a, s16x8 b, f32x16 c) {
    return __builtin_amdgcn_mfma_f32_32x32x16_bf16(__builtin_bit_cast(bf16x8, a), __builtin_bit_cast(bf16x8, b), c, 0, 0, 0);
}
__device__ __forceinline__ void set_m0(unsigned lds) { asm volatile("s_mov_b32 m0, %0" :: "s"(lds)); }
template <int I> __device__ __forceinline__ void glds_o(const char* g, unsigned off) {
    asm volatile("global_load_lds_dwordx4 %1, %0 offset:%2" :: "s"(g), "v"(off), "n"(I * 1024));
}

#define DIAG_WG 0
#define DIAG_T 0
#define ST_DECL unsigned long long dg0_ = __builtin_amdgcn_s_memrealtime(), dg1_ = dg0_
#define ST(i) do { if ((i) == DIAG) { SB(); dg1_ = __builtin_amdgcn_s_memrealtime(); __builtin_amdgcn_s_waitcnt(0xC07F); SB(); } } while (0)
#define ST_CLK(i)
#define ST_FLUSH(wsbase, kid) do { if (blockIdx.x == DIAG_WG && (threadIdx.x >> 6) == (DIAG_T >> 6)) { \
    while (__builtin_amdgcn_s_memrealtime() < dg0_ + 5000ull + DIAG_K * (dg1_ - dg0_)) __builtin_amdgcn_s_sleep(1); } } while (0)
#define ST_ARG
#define ST_PASS
constexpr size_t PG = 2u << 20;
constexpr size_t WS_FLAG  = 0;
constexpr size_t WS_STATS = 32768;
constexpr size_t WS_XNP   = 1 * PG + 4096;
constexpr size_t WS_HF    = 3 * PG + 4096;
constexpr size_t WS_WDF   = 7 * PG + 4096;
constexpr size_t WS_SP    = 8 * PG + 4096;
constexpr size_t WS_END   = 10 * PG;
__host__ __device__ constexpr unsigned hf_tile(int mt) { return (unsigned)WS_HF + (unsigned)(mt >> 5) * (unsigned)PG + (unsigned)(mt & 31) * 57344u; }
constexpr int FL_EPOCH = 0, FL_LN = 32, FL_CS = 64, FL_WD = 96, FL_H = 256  , FL_S = 512  , FL_ARR = 1024  ;

struct Args {
    const u16 *x, *Are, *Aim, *Bre, *Bim, *Cre, *Cim, *logdt, *dskip, *wg, *wu, *wd;
    u16* out;
    char* ws;
};
typedef __amdgpu_buffer_rsrc_t rsrc_t;
__device__ __forceinline__ rsrc_t ws_rsrc(const Args& A) { return __builtin_amdgcn_make_buffer_rsrc(A.ws, 0, (unsigned)WS_END, 0x00020000); }
constexpr int AUX_PUB = 16;
__device__ __forceinline__ void st_pub16(rsrc_t rs, unsigned off, u32x4 v) { __builtin_amdgcn_raw_buffer_store_b128(v, rs, off, 0, AUX_PUB); }
__device__ __forceinline__ void st_pub8(rsrc_t rs, unsigned off, u32x2 v) { __builtin_amdgcn_raw_buffer_store_b64(v, rs, off, 0, AUX_PUB); }
__device__ __forceinline__ u32x4 ld_pub16(rsrc_t rs, unsigned off, unsigned soff = 0) { return __builtin_amdgcn_raw_buffer_load_b128(rs, off, soff, AUX_PUB); }
__device__ __forceinline__ u32x2 ld_pub8(rsrc_t rs, unsigned off) { return __builtin_amdgcn_raw_buffer_load_b64(rs, off, 0, AUX_PUB); }

template <int CTRL> __device__ __forceinline__ float dpp_f(float x) {
    return __builtin_bit_cast(float, __builtin_amdgcn_mov_dpp(__builtin_bit_cast(int, x), CTRL, 0xf, 0xf, true));
}
__device__ __forceinline__ float row16_sum(float x) {
    x += dpp_f<0x128>(x); x += dpp_f<0x124>(x); x += dpp_f<0x122>(x); x += dpp_f<0x121>(x);
    return x;
}
__device__ __forceinline__ float xrow_sum(float x) {
    auto s = __builtin_amdgcn_permlane16_swap(__float_as_uint(x), __float_as_uint(x), false, false);
    x = __uint_as_float(s[0]) + __uint_as_float(s[1]);
    auto t = __builtin_amdgcn_permlane32_swap(__float_as_uint(x), __float_as_uint(x), false, false);
    return __uint_as_float(t[0]) + __uint_as_float(t[1]);
}
struct AuxX { u32x4 x[8]; };
struct AuxW { u32x4 w[16]; };
struct AuxD { u32x4 d[8]; };
__device__ __forceinline__ void aux_load_x(int a, const Args& A, AuxX& in) {
    const int tid = threadIdx.x, lane = tid & 63, wave = tid >> 6;
#pragma unroll
    for (int i = 0; i < 8; ++i)
        in.x[i] = *(const u32x4*)(A.x + (size_t)(a * 64 + wave * 8 + (i >> 2) * 4 + (lane >> 4)) * DM + ((i & 3) * 16 + (lane & 15)) * 8);
}
__device__ __forceinline__ void aux_load_w(int a, const Args& A, AuxW& in) {
    const int tid = threadIdx.x, c = tid & 7, r = tid >> 3;
#pragma unroll
    for (int i = 0; i < 16; ++i) in.w[i] = *(const u32x4*)(((i >> 3) ? A.wu : A.wg) + (size_t)(r + (i & 7) * 64) * FF + a * 64 + c * 8);
}
__device__ __forceinline__ void aux_load_d(int a, const Args& A, AuxD& in) {
    const int tid = threadIdx.x;
#pragma unroll
    for (int i = 0; i < 8; ++i) { const int cc = i * 512 + tid; in.d[i] = *(const u32x4*)(A.wd + (size_t)(a * 64 + (cc >> 6)) * DM + (cc & 63) * 8); }
}
__device__ __forceinline__ float dot2bf(unsigned a, unsigned b, float c) {
    return __builtin_amdgcn_fdot2_f32_bf16(__builtin_bit_cast(bf16x2, a), __builtin_bit_cast(bf16x2, b), c, false);
}
__device__ __forceinline__ void ln_part(int a, const Args& A, const AuxX& in, char* smem) {
    const int tid = threadIdx.x, lane = tid & 63, wave = tid >> 6;
    const rsrc_t rs = ws_rsrc(A);
#pragma unroll
    for (int ps = 0; ps < 2; ++ps) {
        float s0 = 0.f, s1 = 0.f, q0 = 0.f, q1 = 0.f;
#pragma unroll
        for (int i = 0; i < 4; ++i)
#pragma unroll
            for (int j = 0; j < 4; j += 2) {
                const unsigned u0 = in.x[4 * ps + i][j], u1 = in.x[4 * ps + i][j + 1];
                s0 = dot2bf(u0, 0x3f803f80u, s0); s1 = dot2bf(u1, 0x3f803f80u, s1);
                q0 = dot2bf(u0, u0, q0); q1 = dot2bf(u1, u1, q1);
            }
        const float mean = row16_sum(s0 + s1) * (1.f / DM);
        const float var = rbf(fmaxf(row16_sum(q0 + q1) * (1.f / DM) - mean * mean, 0.f));
        const float den = rbf(__builtin_sqrtf(rbf(var + 1e-5f)));
        const float mb = rbf(mean), rd = 1.f / den, nm = -mb * rd;
        const int r = wave * 8 + ps * 4 + (lane >> 4);
        if ((lane & 15) == 0) st_pub8(rs, WS_STATS + (a * 64 + r) * 8, u32x2{__float_as_uint(mb), __float_as_uint(rd)});
#pragma unroll
        for (int i = 0; i < 4; ++i) {
            u32x4 o;
#pragma unroll
            for (int j = 0; j < 4; ++j) {
                const unsigned u = in.x[4 * ps + i][j];
                const f32x2 v = __builtin_elementwise_fma(f32x2{bflo(u), bfhi(u)}, f32x2{rd, rd}, f32x2{nm, nm});
                o[j] = pk2(v[0], v[1]);
            }
            *(u32x4*)(smem + r * 1024 + (((i * 16 + (lane & 15)) ^ ((r >> 2) & 7)) << 4)) = o;
        }
    }
    LBAR();
    const unsigned* T = (const unsigned*)smem;
#pragma unroll
    for (int it = 0; it < 8; ++it) {
        const int dp = it * 32 + wave * 4 + (lane >> 4), tq = lane & 15;
        u32x4 o;
#pragma unroll
        for (int j = 0; j < 4; ++j) o[j] = T[(4 * tq + j) * 256 + (((dp >> 2) ^ (tq & 7)) << 2) + (dp & 3)];
        st_pub16(rs, WS_XNP + (dp * SL + a * 64 + 4 * tq) * 4, o);
    }
}
__device__ __forceinline__ void wd_part(int a, const Args& A, const AuxD& in, char* smem) {
    const int tid = threadIdx.x;
#pragma unroll
    for (int i = 0; i < 8; ++i) {
        const int c = i * 512 + tid, r = c >> 6, cc = c & 63;
        *(u32x4*)(smem + r * 1024 + ((((cc >> 1) ^ (r & 7)) << 1) | (cc & 1)) * 16) = in.d[i];
    }
    LBAR();
    const rsrc_t rs = ws_rsrc(A);
    const int lane = tid & 63, wave = tid >> 6, g = lane >> 4, q = (lane & 15) >> 2, p = lane & 3;
    const unsigned lds0 = (unsigned)(size_t)(LAS char*)smem;
#pragma unroll
    for (int i = 0; i < 8; ++i) {
        const int f = i * 8 + wave, kk = f >> 5, dt = f & 31;
        const int r0 = 32 * kk + 4 * g + q, r1 = r0 + 16;
        const s16x4 lo = __builtin_amdgcn_ds_read_tr16_b64_v4i16((LAS s16x4*)LP(lds0 + r0 * 1024 + ((dt ^ (r0 & 7)) << 5) + p * 8));
        const s16x4 hi = __builtin_amdgcn_ds_read_tr16_b64_v4i16((LAS s16x4*)LP(lds0 + r1 * 1024 + ((dt ^ (r1 & 7)) << 5) + p * 8));
        st_pub16(rs, WS_WDF + ((dt * 56 + 2 * a + kk) * 64 + lane) * 16, __builtin_bit_cast(u32x4, __builtin_shufflevector(lo, hi, 0, 1, 2, 3, 4, 5, 6, 7)));
    }
}

typedef f32x2 cpx;
__device__ __forceinline__ cpx cmul(cpx a, cpx b) {
    cpx r;
    asm("v_pk_mul_f32 %0, %1, %2 op_sel:[0,0] op_sel_hi:[0,1]\n\t"
        "v_pk_fma_f32 %0, %1, %2, %0 op_sel:[1,1,0] op_sel_hi:[1,0,1] neg_lo:[1,0,0]" : "=&v"(r) : "v"(a), "v"(b));
    return r;
}
__device__ __forceinline__ cpx cexp_p(float are2, float airev, float p) {
    const float er = __builtin_amdgcn_exp2f(are2 * p);
    const float rv = __builtin_amdgcn_fractf(airev * p);
    return cpx{er * __builtin_amdgcn_cosf(rv), er * __builtin_amdgcn_sinf(rv)};
}
struct KgIn { u16 are, aim, bre, bim, cre, cim, ldt; };
__device__ __forceinline__ KgIn kg_load(int dp, const Args& A) {
    const int tid = threadIdx.x, lane = tid & 63, wave = tid >> 6;
    const int d = dp * 2 + ((wave >> 1) & 1), i = d * NS + (wave & 1) * 64 + lane;
    KgIn k;
    k.are = A.Are[i]; k.aim = A.Aim[i]; k.bre = A.Bre[i]; k.bim = A.Bim[i]; k.cre = A.Cre[i]; k.cim = A.Cim[i]; k.ldt = A.logdt[d];
    return k;
}
constexpr int KG_TAB = 16 * 81 * 16;
constexpr int KC_OFF = 88 * 1024;
__device__ __forceinline__ void cmul2(cpx& r0, cpx a0, cpx b0, cpx& r1, cpx a1, cpx b1) {
    asm("v_pk_mul_f32 %0, %2, %3 op_sel:[0,0] op_sel_hi:[0,1]\n\t"
        "v_pk_mul_f32 %1, %4, %5 op_sel:[0,0] op_sel_hi:[0,1]\n\t"
        "v_pk_fma_f32 %0, %2, %3, %0 op_sel:[1,1,0] op_sel_hi:[1,0,1] neg_lo:[1,0,0]\n\t"
        "v_pk_fma_f32 %1, %4, %5, %1 op_sel:[1,1,0] op_sel_hi:[1,0,1] neg_lo:[1,0,0]"
        : "=&v"(r0), "=&v"(r1) : "v"(a0), "v"(b0), "v"(a1), "v"(b1));
}
struct KgVals { unsigned v[41]; };
__device__ __forceinline__ void kgen_values(const KgIn& in, KgVals& o) {
    const int wave = __builtin_amdgcn_readfirstlane(threadIdx.x >> 6);
    const float dt = rbf(expf(bf2f(in.ldt)));
    const float are2 = dt * bf2f(in.are) * 1.44269504088896341f, airev = dt * bf2f(in.aim) * 0.15915494309189535f;
    auto put = [&](int i, cpx v) { o.v[i] = pk2(v[0], v[1]); };
    if (wave < 4) {
        cpx P[16];
        P[0] = cpx{1.f, 0.f};
        P[1] = cexp_p(are2, -airev, 1.f);
        P[2] = cmul(P[1], P[1]);
        cmul2(P[3], P[2], P[1], P[4], P[2], P[2]);
        cmul2(P[5], P[4], P[1], P[6], P[3], P[3]);
        cmul2(P[7], P[6], P[1], P[8], P[4], P[4]);
        cmul2(P[9], P[8], P[1], P[10], P[5], P[5]);
        cmul2(P[11], P[10], P[1], P[12], P[6], P[6]);
        cmul2(P[13], P[12], P[1], P[14], P[7], P[7]);
        P[15] = cmul(P[14], P[1]);
#pragma unroll
        for (int b = 0; b < 16; ++b) put(b, P[b]);
#pragma unroll
        for (int s = 0; s < 3; ++s)
#pragma unroll
            for (int i = 0; i < 8; i += 2) {
                cpx a0 = P[8 + i], a1 = P[9 + i];
                cmul2(P[8 + i], a0, a0, P[9 + i], a1, a1);
                put(16 + 8 * s + i, P[8 + i]); put(17 + 8 * s + i, P[9 + i]);
            }
        o.v[40] = 0;
    } else {
        const cpx dB = {dt * bf2f(in.bre), dt * bf2f(in.bim)};
        const cpx w = cmul(cpx{bf2f(in.cre), bf2f(in.cim)}, dB);
        const cpx z16 = cexp_p(are2, airev, 16.f), z128 = cexp_p(are2, airev, 128.f);
        cpx z32, z64, z256, z512, p0, p1;
        cmul2(z32, z16, z16, z256, z128, z128);
        cmul2(z64, z32, z32, z512, z256, z256);
        const cpx z1024 = cmul(z512, z512);
        p0 = w; p1 = cmul(w, z16);
#pragma unroll
        for (int a = 0; a < 16; a += 2) {
            put(a, p0); put(1 + a, p1);
            if (a < 14) { cpx q0 = p0, q1 = p1; cmul2(p0, q0, z32, p1, q1, z32); }
        }
        cmul2(p0, w, z256, p1, w, z512);
#pragma unroll
        for (int a = 0; a < 8; ++a) {
            put(16 + a, p0); put(24 + a, p1);
            if (a < 7) { cpx q0 = p0, q1 = p1; cmul2(p0, q0, z32, p1, q1, z64); }
        }
        p0 = cmul(w, z1024); p1 = cmul(p0, z128);
#pragma unroll
        for (int a = 0; a < 8; a += 2) {
            put(32 + a, p0); put(33 + a, p1);
            cpx q0 = p0, q1 = p1; cmul2(p0, q0, z256, p1, q1, z256);
        }
        put(40, p0);
    }
#pragma unroll
    for (int i = 0; i < 41; ++i) asm volatile("" : "+v"(o.v[i]));
}
__device__ __forceinline__ void kgen_tables(const KgVals& in, char* smem) {
    const int tid = threadIdx.x, lane = tid & 63, wave = __builtin_amdgcn_readfirstlane(tid >> 6);
    unsigned* tab = (unsigned*)(smem + (wave & 3) * KG_TAB) + (lane >> 2) * (81 * 4) + (lane & 3) + (wave >> 2) * (40 * 4);
#pragma unroll
    for (int i = 0; i < 40; ++i) tab[i * 4] = in.v[i];
    if (wave >= 4) tab[40 * 4] = in.v[40];
    LBAR();
    {
        const int ch = wave >> 2, s = wave & 3, b = lane & 15, g = lane >> 4;
        const int ra = s == 0 ? 40 + b : (s == 3 ? 72 + min(b, 8) : 48 + 8 * s + (b & 7));
        const int sh = min(s, b == 0 ? 3 : __builtin_clz((unsigned)b) - 28), bb = b << sh, e = s - sh;
        const int rb = e > 0 ? 16 + 8 * (e - 1) + (bb - 8) : bb;
        const char* ta = smem + ch * 2 * KG_TAB + (g * 81 + ra) * 16;
        const char* tb = smem + ch * 2 * KG_TAB + (g * 81 + rb) * 16;
        f32x4 acc = {0, 0, 0, 0};
#pragma unroll
        for (int k = 0; k < 8; ++k)
            acc = mfma16(*(const s16x8*)(ta + (k >> 2) * KG_TAB + (k & 3) * (4 * 81 * 16)), *(const s16x8*)(tb + (k >> 2) * KG_TAB + (k & 3) * (4 * 81 * 16)), acc);
        float* kc = (float*)(smem + KC_OFF) + ch * 656 + (s ? 128 * (s + 1) : 0) + 64 * g + b;
        if (s == 0 || g < 2) { kc[0] = acc[0]; kc[16] = acc[1]; kc[32] = acc[2]; kc[48] = acc[3]; }
        else if (s == 3 && g == 2) kc[0] = acc[0];
    }
}

constexpr int KR_C = 2087;
constexpr int CV_KSTR = 4416;
constexpr int CV_KB = 4 * CV_KSTR;
constexpr int CV_UB = 96 * 80;
constexpr int CV_U = 36 * 1024;
constexpr int CV_X = 52 * 1024;
constexpr int G2_S = 52 * 1024;
constexpr int G2_P = 94 * 1024;
constexpr int CTL_OFF = 158 * 1024;
struct CvIn { u32x4 u; u16 ds; };
__device__ __forceinline__ void conv_uimage(const CvIn& in, char* smem) {
    const int tid = threadIdx.x;
    const int idx = 1024 + tid * 4, off = (idx >> 5) * 80 + (idx & 31) * 2;
    *(u32x2*)(smem + CV_U + off) = u32x2{__builtin_amdgcn_perm(in.u[1], in.u[0], 0x05040100), __builtin_amdgcn_perm(in.u[3], in.u[2], 0x05040100)};
    *(u32x2*)(smem + CV_U + CV_UB + off) = u32x2{__builtin_amdgcn_perm(in.u[1], in.u[0], 0x07060302), __builtin_amdgcn_perm(in.u[3], in.u[2], 0x07060302)};
    if (tid < 256) {
        const int ch = tid >> 7, zi = (tid & 127) * 8;
        *(u32x4*)(smem + CV_U + ch * CV_UB + (zi >> 5) * 80 + (zi & 31) * 2) = u32x4{0, 0, 0, 0};
    }
}
struct KIdx { unsigned p[6]; };
__device__ __forceinline__ void conv_kindex(KIdx& o) {
    const int i = threadIdx.x & 255, top = 2050 - 8 * i;
#pragma unroll
    for (int e = 0; e < 12; e += 2) {
        const int l0 = min(top - e, SL - 1), l1 = min(max(top - e - 1, 0), SL - 1);
        const unsigned bb = pk2((float)l0, (float)l1);
        const unsigned i0 = l0 < 128 ? l0 : (bb & 0xffffu) - 17024u, i1 = l1 < 128 ? l1 : (bb >> 16) - 17024u;
        o.p[e >> 1] = (i0 << 2) | (i1 << 18);
        asm volatile("" : "+v"(o.p[e >> 1]));
    }
}
__device__ __forceinline__ void conv_kimage(const KIdx& ix, char* smem) {
    const int tid = threadIdx.x, ch = tid >> 8, i = tid & 255;
    const char* kc = smem + KC_OFF + ch * 656 * 4;
    float v[11];
#pragma unroll
    for (int e = 0; e < 12; e += 2) {
        v[e] = *(const float*)(kc + (ix.p[e >> 1] & 0xffffu));
        if (e + 1 < 11) v[e + 1] = *(const float*)(kc + (ix.p[e >> 1] >> 16));
    }
    if (i == 0) { v[0] = 0.f; v[1] = 0.f; v[2] = 0.f; }
    unsigned ev[5], od[5];
#pragma unroll
    for (int k = 0; k < 5; ++k) { ev[k] = pk2(v[2 * k], v[2 * k + 1]); od[k] = pk2(v[2 * k + 1], v[2 * k + 2]); }
    char* dst = smem + ch * CV_KB + (i + 5) * 16;
    *(u32x4*)(dst) = u32x4{od[1], od[2], od[3], od[4]};
    *(u32x4*)(dst + CV_KSTR) = u32x4{ev[1], ev[2], ev[3], ev[4]};
    *(u32x4*)(dst + 2 * CV_KSTR) = u32x4{od[0], od[1], od[2], od[3]};
    *(u32x4*)(dst + 3 * CV_KSTR) = u32x4{ev[0], ev[1], ev[2], ev[3]};
    if (i == 255) {
        char* e = smem + ch * CV_KB + 261 * 16;
        *(u32x4*)(e) = u32x4{0, 0, 0, 0};
        *(u32x4*)(e + CV_KSTR) = u32x4{pk2(v[10], 0.f), 0, 0, 0};
        *(u32x4*)(e + 2 * CV_KSTR) = u32x4{od[4], 0, 0, 0};
        *(u32x4*)(e + 3 * CV_KSTR) = u32x4{ev[4], pk2(v[10], 0.f), 0, 0};
    }
    if (i < 32) *(u32x4*)(smem + ch * CV_KB + (i >> 3) * CV_KSTR + ((i & 7) < 5 ? (i & 7) : 257 + (i & 7)) * 16) = u32x4{0, 0, 0, 0};
}
template <int Q0, int Q1, int NTILE>
__device__ __forceinline__ void conv_span(unsigned fa, unsigned ua, f32x16& acc0, f32x16& acc1) {
    constexpr int N = Q1 - Q0 + 1, PD = NTILE == 2 ? 4 : 6;
    const unsigned fb = fa - 32 * Q1, ub = ua - ((Q1 + 1) / 2) * 80;
    unsigned fb2 = fb + 8;
    asm volatile("" : "+v"(fb2));
    u32x2 flo[PD], fhi[PD];
    s16x8 b0[PD], b1[PD];
    auto ld = [&](int q, int sl) {
        const int fo = 32 * (Q1 - q);
        flo[sl] = *(const LAS u32x2*)(LP(fb) + fo); fhi[sl] = *(const LAS u32x2*)(LP(fb2) + fo);
        const int qq = q & 1, blk = (q + qq) / 2;
        const int uo = ((Q1 + 1) / 2 - blk) * 80 + 32 * qq;
        if (NTILE == 2) b0[sl] = *(const LAS s16x8*)(LP(ub) + uo);
        b1[sl] = *(const LAS s16x8*)(LP(ub) + uo + 32 * 80);
    };
#pragma unroll
    for (int i = 0; i < PD; ++i) if (i < N) ld(Q0 + i, i);
    SB();
#pragma unroll
    for (int i = 0; i < N; ++i) {
        const int sl = i % PD;
        const s16x8 F = __builtin_bit_cast(s16x8, u32x4{flo[sl][0], flo[sl][1], fhi[sl][0], fhi[sl][1]});
        const s16x8 x0 = b0[sl], x1 = b1[sl];
        if (NTILE == 2) acc0 = mfma32(F, x0, acc0);
        acc1 = mfma32(F, x1, acc1);
        if (i + PD < N) ld(Q0 + i + PD, sl);
        SB();
    }
}
__device__ __forceinline__ void conv_mma(int wave, char* smem, f32x16& acc0, f32x16& acc1) {
    const int lane = threadIdx.x & 63;
    const unsigned lds0 = (unsigned)(size_t)(LAS char*)smem;
    const int ch = (wave >> 1) & 1, part = (wave < 4 ? 2 : 0) + (wave & 1);
    const int r = lane & 31, h = lane >> 5, S = (r + 1) & 3;
    const unsigned fa = lds0 + ch * CV_KB + S * CV_KSTR + (KR_C - r + 8 * h + S) * 2;
    const unsigned ua = lds0 + CV_U + ch * CV_UB + (32 + r) * 80 + 16 * h;
#pragma unroll
    for (int i = 0; i < 16; ++i) { acc0[i] = 0.f; acc1[i] = 0.f; }
    if (part == 0) conv_span<-1, 23, 2>(fa, ua, acc0, acc1);
    else if (part == 1) conv_span<24, 47, 2>(fa, ua, acc0, acc1);
    else if (part == 2) { conv_span<48, 64, 2>(fa, ua, acc0, acc1); conv_span<65, 79, 1>(fa, ua, acc0, acc1); }
    else conv_span<80, 128, 1>(fa, ua, acc0, acc1);
}
__device__ __forceinline__ unsigned lds_ld(unsigned addr) { return __hip_atomic_load((LAS unsigned*)LP(addr), __ATOMIC_RELAXED, __HIP_MEMORY_SCOPE_WORKGROUP); }
__device__ __forceinline__ void lds_st(unsigned addr, unsigned v) { __hip_atomic_store((LAS unsigned*)LP(addr), v, __ATOMIC_RELAXED, __HIP_MEMORY_SCOPE_WORKGROUP); }
__device__ __forceinline__ unsigned lds_add(unsigned addr) { return __hip_atomic_fetch_add((LAS unsigned*)LP(addr), 1u, __ATOMIC_RELAXED, __HIP_MEMORY_SCOPE_WORKGROUP); }
__device__ __forceinline__ void put_tile(char* slot, const f32x16& a) {
    f32x4* X = (f32x4*)slot + (threadIdx.x & 63);
#pragma unroll
    for (int i = 0; i < 4; ++i) X[i * 64] = f32x4{a[4 * i], a[4 * i + 1], a[4 * i + 2], a[4 * i + 3]};
}
__device__ __forceinline__ void add_tile(f32x16& y, const char* slot) {
    const f32x4* X = (const f32x4*)slot + (threadIdx.x & 63);
#pragma unroll
    for (int i = 0; i < 4; ++i) {
        const f32x4 p = X[i * 64];
#pragma unroll
        for (int e = 0; e < 4; ++e) y[4 * i + e] += p[e];
    }
}
__device__ __forceinline__ void conv_handover(int wave, char* smem, const f32x16& acc0, const f32x16& acc1) {
    const unsigned lds0 = (unsigned)(size_t)(LAS char*)smem;
    const int ch = (wave >> 1) & 1;
    char* X = smem + CV_X + ch * 20480;
    if (wave >= 4) { put_tile(X + (wave & 1) * 4096, acc0); put_tile(X + (2 + (wave & 1)) * 4096, acc1); }
    else if ((wave & 1) == 0) put_tile(X + 4 * 4096, acc1);
    if (wave >= 4 || (wave & 1) == 0) {
        wait_lgkm0();
        if ((threadIdx.x & 63) == 0) lds_add(lds0 + CTL_OFF + 8 + ch * 4);
    }
}
__device__ __forceinline__ void conv_finish(int wave, int dp, const Args& A, u16 dsk, char* smem, const f32x16& acc0, const f32x16& acc1) {
    const int lane = threadIdx.x & 63;
    const unsigned lds0 = (unsigned)(size_t)(LAS char*)smem;
    const int ch = (wave >> 1) & 1, o = wave & 1, r = lane & 31, h = lane >> 5;
    while (__builtin_amdgcn_readfirstlane(lds_ld(lds0 + CTL_OFF + 8 + ch * 4)) < 3) __builtin_amdgcn_s_sleep(1);
    asm volatile("" ::: "memory");
    const char* X = smem + CV_X + ch * 20480;
    f32x16 y;
    if (o == 0) { y = acc0; add_tile(y, X); add_tile(y, X + 4096); }
    else { y = acc1; add_tile(y, X + 2 * 4096); add_tile(y, X + 3 * 4096); add_tile(y, X + 4 * 4096); }
    const float ds = bf2f(dsk);
    char* S = smem + CV_X + ch * 20480 + o * 2 * 4096;
    const char* U = smem + CV_U + ch * CV_UB + (32 + 32 * o + r) * 80 + 8 * h;
#pragma unroll
    for (int i = 0; i < 4; ++i) {
        const u32x2 uu = *(const u32x2*)(U + 16 * i);
        const f32x2 y0 = f32x2{y[4 * i], y[4 * i + 1]} + f32x2{bflo(uu[0]), bfhi(uu[0])} * ds;
        const f32x2 y1 = f32x2{y[4 * i + 2], y[4 * i + 3]} + f32x2{bflo(uu[1]), bfhi(uu[1])} * ds;
        *(u32x2*)(S + r * 64 + ((i ^ ((r >> 1) & 3)) << 4) + h * 8) = u32x2{pk2(y0[0], y0[1]), pk2(y1[0], y1[1])};
    }
    wait_lgkm0();
    const rsrc_t rs = ws_rsrc(A);
#pragma unroll
    for (int i = 0; i < 2; ++i)
        st_pub16(rs, WS_SP + ((dp * 2 + ch) * SL + 1024 * o) * 2 + i * 1024 + lane * 16, *(const u32x4*)(S + i * 1024 + (lane >> 2) * 64 + (((lane & 3) ^ ((lane >> 3) & 3)) << 4)));
}
typedef unsigned long long u64;
__device__ __forceinline__ void st_flag(u64* p, u64 v) { __hip_atomic_store((GAS u64*)p, v, __ATOMIC_RELAXED, __HIP_MEMORY_SCOPE_AGENT); }
__device__ __forceinline__ u64 ld_flag(const u64* p) { return __hip_atomic_load((GAS u64*)p, __ATOMIC_RELAXED, __HIP_MEMORY_SCOPE_AGENT); }
#define DBG_TMO(code)
#define DBG_ARG
#define DBG_PASS(c)
template <int SLEEP = 4>
__device__ __forceinline__ void wait_flags(const u64* p, u64 tag DBG_ARG) {
    const u64 t0 = __builtin_amdgcn_s_memrealtime();
    while (!__all(ld_flag(p) == tag)) {
        if (__builtin_amdgcn_s_memrealtime() - t0 > 300000ull) { DBG_TMO(code); break; }
        __builtin_amdgcn_s_sleep(SLEEP);
    }
    asm volatile("" ::: "memory");
}
template <int SLEEP = 4>
__device__ __forceinline__ void team_wait_flags(bool poller, unsigned lds_word, const u64* p, u64 tag DBG_ARG) {
    if (poller) {
        wait_flags<SLEEP>(p, tag);
        if ((threadIdx.x & 63) == 0) lds_st(lds_word, 1u);
    } else {
        while (__builtin_amdgcn_readfirstlane(lds_ld(lds_word)) == 0) __builtin_amdgcn_s_sleep(4);
        asm volatile("" ::: "memory");
    }
}
__device__ __forceinline__ void poll_issue(const u64* p) { asm volatile("global_load_dwordx2 v[254:255], %0, off sc1" :: "v"(p) : "v254", "v255"); }
template <int SLEEP = 4>
__device__ __forceinline__ void poll_check(const u64* p, u64 tag DBG_ARG) {
    unsigned lo, hi;
    asm volatile("s_waitcnt vmcnt(0)\n\tv_mov_b32 %0, v254\n\tv_mov_b32 %1, v255" : "=v"(lo), "=v"(hi) :: "memory");
    const u64 v = ((u64)hi << 32) | lo;
    if (!__all(v == tag)) wait_flags<SLEEP>(p, tag);
}
template <int OFF> __device__ __forceinline__ void glds_dw(const void* p) {
    asm volatile("global_load_lds_dword %0, off offset:%1" :: "v"((const char*)p - OFF), "n"(OFF));
}
template <int OFF> __device__ __forceinline__ void glds_dw_sc1(const void* p) {
    asm volatile("global_load_lds_dword %0, off offset:%1 sc1" :: "v"((const char*)p - OFF), "n"(OFF));
}
constexpr int DUMMY_OFF = CTL_OFF + 512;
__device__ __forceinline__ void touch(unsigned lds0, const void* p) { set_m0(lds0 + DUMMY_OFF); asm volatile("s_nop 0"); glds_dw_sc1<0>(p); }
__device__ __forceinline__ unsigned kg_area(int wave) { return 32 * 1024 + wave * 2048; }
__device__ __forceinline__ void kg_issue(int dp, const Args& A, unsigned lds0) {
    const int tid = threadIdx.x, lane = tid & 63, wave = __builtin_amdgcn_readfirstlane(tid >> 6);
    const int d = dp * 2 + ((wave >> 1) & 1), i = d * NS + 2 * lane;
    set_m0(lds0 + kg_area(wave));
    asm volatile("s_nop 0");
    glds_dw<0>(A.Are + i); glds_dw<256>(A.Aim + i); glds_dw<512>(A.Bre + i); glds_dw<768>(A.Bim + i);
    glds_dw<1024>(A.Cre + i); glds_dw<1280>(A.Cim + i); glds_dw<1536>(A.logdt + (d & ~1));
    glds_dw_sc1<7 * 256>((const char*)(A.ws + WS_FLAG) + FL_EPOCH * 8 + (lane & 1) * 4);
}
__device__ __forceinline__ KgIn kg_read(const char* smem, u64& tag) {
    const int tid = threadIdx.x, lane = tid & 63, wave = __builtin_amdgcn_readfirstlane(tid >> 6);
    const char* ar = smem + kg_area(wave);
    const u16* pm = (const u16*)ar + (wave & 1) * 64 + lane;
    KgIn r;
    r.are = pm[0]; r.aim = pm[128]; r.bre = pm[256]; r.bim = pm[384]; r.cre = pm[512]; r.cim = pm[640];
    r.ldt = ((const u16*)(ar + 1536))[(wave >> 1) & 1];
    tag = *(const u64*)(ar + 1792) + 1;
    wait_lgkm0();
    return r;
}

constexpr int XS = 16384, WSL = 16384, NXS = 3, NWS = 6;
constexpr int CS_OFF = CTL_OFF + 1024;
constexpr int FUSED_LDS = 160 * 1024;
#define SWAIT0 48
#define SWAIT1 24
#define CVPRIO 2
#define CSM 1
#define NO_CS 0
#define G2PD 5
#define TLBW 0
#define TLBW_CODE_PAGES 6
#define TLBW_BACK 0

__device__ __forceinline__ void tlb_warm(const Args& A, unsigned lds0) {
    const int lane = threadIdx.x & 63;
    unsigned long long pc;
    asm volatile("s_getpc_b64 %0" : "=s"(pc));
    const char* pa;
    if (lane < 16) pa = (const char*)(pc & ~4095ull) + min(lane, TLBW_CODE_PAGES) * 4096 - TLBW_BACK;
    else if (lane < 26) { const int pg = max(lane - 16, 1); pa = A.ws + (size_t)pg * PG + (pg == 2 || pg == 9 ? 8192 : 3968); }
    else if (lane < 28) pa = (const char*)A.out + (lane - 26) * (1u << 20);
    else if (lane == 28) pa = (const char*)A.dskip;
    else pa = (const char*)A.wd + min(lane - 29, 1) * (1u << 20);
    set_m0(lds0 + DUMMY_OFF);
    asm volatile("s_nop 0");
    glds_dw<0>(pa);
}

__global__ __launch_bounds__(512, 2) void k_fused(Args A) {
    extern __shared__ __attribute__((aligned(16))) char smem[];
    ST_DECL; ST(0);
    const int tid = threadIdx.x, lane = tid & 63, wave = __builtin_amdgcn_readfirstlane(tid >> 6);
    const int b = blockIdx.x, xcd = b & 7, ix = b >> 3;
    const int dp = b;
    const int rb2 = xcd * 4 + (ix & 3), cb2 = ix >> 2;
    u64* fl = (u64*)(A.ws + WS_FLAG);
    const rsrc_t rs = ws_rsrc(A);
    const unsigned lds0 = (unsigned)(size_t)(LAS char*)smem;
    u64 tag;
    CvIn cin;

    KgVals kv;
    KIdx kix;
    if (ix < 28) {
        const int rb = (xcd & 3) * 4 + (ix & 3), cb = (xcd >> 2) * 7 + (ix >> 2);
        const int wm = wave & 1, wn = wave >> 1;
        unsigned xoff[2], woff[2];
#pragma unroll
        for (int i = 0; i < 2; ++i) {
            const int ci = (wave * 2 + i) * 64 + lane, row = ci >> 3, pos = ci & 7, ch = pos ^ ((row >> 1) & 7);
            xoff[i] = ((rb * 128 + row) * DM + ch * 8) * 2 - i * 1024 + 4096;
        }
#pragma unroll
        for (int i = 0; i < 2; ++i) {
            const int ci = ((wave & 3) * 2 + i) * 64 + lane, k = ci >> 4, pos = ci & 15;
            const int f = (k & 3) | (((k >> 3) & 1) << 2);
            const int c = (((pos >> 1) ^ f) << 1) | (pos & 1);
            woff[i] = (k * FF + cb * 128 + c * 8) * 2 - i * 1024 + 4096;
        }
        const char* xb = (const char*)A.x - 4096;
        const char* wb = (const char*)((wave >> 2) ? A.wu : A.wg) - 4096;
        const unsigned xdst = lds0 + wave * 2048, wdst = lds0 + NXS * XS + wave * 2048;
        auto m0x = [&](int X) { set_m0(xdst + (X % NXS) * XS); };
        auto m0w = [&](int j) { set_m0(wdst + (j % NWS) * WSL); };
#define LDX(X, I) glds_o<I>(xb + (X) * 128, xoff[I])
#define LDW(J, I) glds_o<I>(wb + (size_t)(J) * (32 * FF * 2), woff[I])
#define LDX2(X) do { m0x(X); asm volatile("s_nop 0"); LDX(X, 0); LDX(X, 1); } while (0)
#define LDW2(J) do { m0w(J); asm volatile("s_nop 0"); LDW(J, 0); LDW(J, 1); } while (0)
        LDX2(0); LDW2(0);
        kg_issue(dp, A, lds0);
        LDW2(1); LDX2(1); LDW2(2); LDW2(3); LDW2(4);
        if (TLBW && wave == 7) tlb_warm(A, lds0);
        SB();
        conv_kindex(kix);
        const int m = lane & 15, g = lane >> 4, q = (lane & 15) >> 2, p = lane & 3;
        const int fx = (m >> 1) & 7;
        int xrd[2];
#pragma unroll
        for (int ks = 0; ks < 2; ++ks) xrd[ks] = lds0 + (wm * 64 + m) * 128 + (((4 * ks + g) ^ fx) << 4);
        const int fw = q | ((g & 1) << 2);
        int wrd[2][4];
#pragma unroll
        for (int f = 0; f < 4; ++f) {
            wrd[0][f] = lds0 + NXS * XS + (8 * g + q) * 256 + (((wn * 2 + (f & 1)) ^ fw) << 5) + p * 8 + ((f >> 1) ^ wm) * 8192;
            wrd[1][f] = wrd[0][f] + 3 * WSL;
        }
        const char* pfa;
        if (tid < 352) { const int idx = (ix & 3) * 352 + tid, mat = idx >= 704, rem = idx - 704 * mat;
            pfa = (const char*)(mat ? A.wu : A.wg) + (size_t)(160 + (rem >> 1)) * (FF * 2) + cb * 256 + (rem & 1) * 128; }
        else { const int idx = min((ix >> 2) * 110 + min(tid - 352, 109), 767), row = idx / 6;
            pfa = (const char*)A.x + (size_t)(rb * 128 + row) * 1024 + (2 + idx - 6 * row) * 128; }
        if (TLBW && wave == 7) wait_vm<11>(); else wait_vm<10>();
        {
            const KgIn kin = kg_read(smem, tag);
            ST(1);
            kgen_values(kin, kv);
        }
        f32x4 acc[4][4];
#pragma unroll
        for (int i = 0; i < 4; ++i)
#pragma unroll
            for (int j = 0; j < 4; ++j) acc[i][j] = f32x4{0, 0, 0, 0};
        f32x4 csa[2] = {f32x4{0, 0, 0, 0}, f32x4{0, 0, 0, 0}};
        const s16x8 ones = {0x3f80, 0x3f80, 0x3f80, 0x3f80, 0x3f80, 0x3f80, 0x3f80, 0x3f80};
        s16x8 xf[2][4];
        s16x4 wlo[4], whi[4];
        auto rdx = [&](int buf, int j, int mt) { xf[buf][mt] = *(const LAS s16x8*)(LP(xrd[j & 1]) + ((j >> 1) % NXS) * XS + mt * 2048); };
        auto rdwl = [&](int j, int f) {
            const int slot = j % NWS;
            wlo[f] = __builtin_amdgcn_ds_read_tr16_b64_v4i16((LAS s16x4*)(LP(wrd[slot >= 3][f]) + (slot % 3) * WSL));
        };
        auto rdwh = [&](int j, int f) {
            const int slot = j % NWS;
            whi[f] = __builtin_amdgcn_ds_read_tr16_b64_v4i16((LAS s16x4*)(LP(wrd[slot >= 3][f]) + (slot % 3) * WSL + 1024));
        };
        BAR();
        ST(2);
        if (PF) touch(lds0, pfa);
#pragma unroll
        for (int mt = 0; mt < 4; ++mt) rdx(0, 0, mt);
#pragma unroll
        for (int f = 0; f < 4; ++f) { rdwl(0, f); rdwh(0, f); }
        SB();
        const u64* pl = fl + FL_LN + (lane & 31);
#pragma unroll
        for (int j = 0; j < 16; ++j) {
            if (j < 15) {
                auto iss = [](int w) { return w < 0 ? 0 : ((w + 5 < 16 ? 2 : 0) + ((w & 1) == 0 && w / 2 + 2 < 8 ? 2 : 0)); };
                int allow;
                if (j == 0) allow = 8 + PF;
                else if (j == 1) allow = 4 + PF + iss(0);
                else if (j == 2) allow = 2 + PF + iss(0) + iss(1);
                else if (j == 3) allow = iss(1) + iss(2);
                else allow = (j & 1) ? iss(j - 2) + iss(j - 1) : iss(j - 3) + iss(j - 2) + iss(j - 1);
                if (j == 14 && wave == 0) wait_vm<1>();
                else if (TLBW && j <= 2 && wave == 7) {
                    if (allow + 1 == 9) wait_vm<9>(); else if (allow + 1 == 10) wait_vm<10>(); else if (allow + 1 == 11) wait_vm<11>(); else wait_vm<0>();
                }
                else switch (allow) {
#define WV(n) case n: wait_vm<n>(); break;
                    WV(0) WV(1) WV(2) WV(3) WV(4) WV(5) WV(6) WV(7) WV(8) WV(9) WV(10) WV(11) WV(12)
#undef WV
                    default: wait_vm<0>(); break;
                }
                BAR();
                SB();
            }
            if (j == 13) { if (wave == 0) poll_issue(pl); SB(); }
            const int cur = j & 1, nxt = cur ^ 1;
            const bool rd = j < 15, lw = j + 5 < 16, lx = (j & 1) == 0 && j / 2 + 2 < 8;
#pragma unroll
            for (int f = 0; f < 4; ++f) {
                const s16x8 wf = __builtin_shufflevector(wlo[f], whi[f], 0, 1, 2, 3, 4, 5, 6, 7);
                acc[f][0] = mfma16(wf, xf[cur][0], acc[f][0]);
                if (rd) rdx(nxt, j + 1, f);
                SB();
                acc[f][1] = mfma16(wf, xf[cur][1], acc[f][1]);
                if (f == 0 && lx) m0x(j / 2 + 2);
                if (f == 1 && lx) LDX(j / 2 + 2, 0);
                if (f == 2 && lw) m0w(j + 5);
                if (f == 3 && lw) LDW(j + 5, 0);
                if (f < 2) csa[f] = mfma16(wf, ones, csa[f]);
                SB();
                acc[f][2] = mfma16(wf, xf[cur][2], acc[f][2]);
                if (rd) rdwl(j + 1, f);
                SB();
                acc[f][3] = mfma16(wf, xf[cur][3], acc[f][3]);
                if (rd) rdwh(j + 1, f);
                if (f == 1 && lx) LDX(j / 2 + 2, 1);
                if (f == 3 && lw) LDW(j + 5, 1);
                SB();
            }
        }
#undef LDX
#undef LDW
#undef LDX2
#undef LDW2
        ST(3);
        if (wave == 0) poll_check<8>(pl, tag DBG_PASS(1));
        if (tid < 8) *(unsigned*)(smem + CTL_OFF + tid * 4) = 0;
        if (m == 0) { f32x4* c = (f32x4*)(smem + CS_OFF) + (wn * 4 + 2 * wm) * 4 + g; c[0] = csa[0]; c[4] = csa[1]; }
        LBAR();
        u32x2 stt[4];
        f32x4 csv[4];
#pragma unroll
        for (int mt = 0; mt < 4; ++mt) stt[mt] = ld_pub8(rs, WS_STATS + (rb * 128 + wm * 64 + 16 * mt + m) * 8);
#pragma unroll
        for (int f = 0; f < 4; ++f) csv[f] = *(const f32x4*)(smem + CS_OFF + ((wn * 4 + f) * 16 + 4 * g) * 4);
        cin.u = ld_pub16(rs, WS_XNP + (dp * SL + tid * 4) * 4);
        cin.ds = A.dskip[dp * 2 + ((tid >> 7) & 1)];
        SB();
        kgen_tables(kv, smem);
        ST(4);
        const int kt = cb * 4 + wn;
        auto epi = [&](auto GS) __attribute__((always_inline)) {
            constexpr int gs = decltype(GS)::value, us = 2 - gs;
#pragma unroll
            for (int mt = 0; mt < 4; ++mt) {
                const float mb = __uint_as_float(stt[mt][0]), rd = __uint_as_float(stt[mt][1]);
                const float nl = rd * -1.44269504088896341f, ird2 = __builtin_amdgcn_rcpf(rd * rd);
                u32x4 o;
#pragma unroll
                for (int u = 0; u < 2; ++u)
#pragma unroll
                    for (int r = 0; r < 2; ++r) {
                        const f32x2 gp = f32x2{acc[gs + u][mt][2 * r], acc[gs + u][mt][2 * r + 1]} - mb * f32x2{csv[u][2 * r], csv[u][2 * r + 1]};
                        const f32x2 up = f32x2{acc[us + u][mt][2 * r], acc[us + u][mt][2 * r + 1]} - mb * f32x2{csv[2 + u][2 * r], csv[2 + u][2 * r + 1]};
                        const f32x2 t = gp * nl;
                        const f32x2 e = {__builtin_amdgcn_exp2f(t[0]), __builtin_amdgcn_exp2f(t[1])};
                        const f32x2 d = __builtin_elementwise_fma(e, f32x2{ird2, ird2}, f32x2{ird2, ird2});
                        const f32x2 h = gp * up * f32x2{__builtin_amdgcn_rcpf(d[0]), __builtin_amdgcn_rcpf(d[1])};
                        o[2 * u + r] = pk2(h[0], h[1]);
                    }
                st_pub16(rs, hf_tile(rb * 8 + wm * 4 + mt) + kt * 1024 + lane * 16, o);
            }
        };
        if (wm) epi(std::integral_constant<int, 2>{}); else epi(std::integral_constant<int, 0>{});
        if (lane == 0) st_flag(fl + FL_ARR + 8 * b + wave, tag);
        if (wave == 0) poll_issue(fl + FL_WD + min(lane, 27));
        LBAR();
        ST(5);
        conv_uimage(cin, smem);
        conv_kimage(kix, smem);
        if (wave == 0) poll_check<8>(fl + FL_WD + min(lane, 27), tag DBG_PASS(6));
        wait_vm<0>();
        LBAR();
        if (tid == 0) st_flag(fl + FL_H + rb * 16 + cb, tag);
        touch(lds0, A.ws + WS_WDF + (size_t)cb2 * 4 * 57344 + (size_t)((ix & 3) * 448 + min(tid, 447)) * 128);
    } else {
        const int a = (ix - 28) * 8 + xcd;
        AuxX ax;
        kg_issue(dp, A, lds0);
        aux_load_x(a, A, ax);
        SB();
        cin.ds = A.dskip[dp * 2 + ((tid >> 7) & 1)];
        wait_vm<0>();
        const KgIn kin = kg_read(smem, tag);
        if (lane == 0) st_flag(fl + FL_ARR + 8 * b + wave, tag);
        if (tid < 8) *(unsigned*)(smem + CTL_OFF + tid * 4) = 0;
        LBAR();
        ln_part(a, A, ax, smem);
        ST(1);
        if (a < 28) {
            AuxD ad;
            SB();
            aux_load_d(a, A, ad);
            SB();
            wait_vm<8>();
            LBAR();
            if (tid == 0) st_flag(fl + FL_LN + a, tag);
            ST(2);
            wd_part(a, A, ad, smem);
            wait_vm<0>();
            LBAR();
            if (tid == 0) st_flag(fl + FL_WD + a, tag);
        } else {
            wait_vm<0>();
            LBAR();
            if (tid == 0) st_flag(fl + FL_LN + a, tag);
        }
        ST(3);
        conv_kindex(kix);
        kgen_values(kin, kv);
        kgen_tables(kv, smem);
        ST(4);
        if (wave == 0) wait_flags<8>(fl + (lane < 32 ? FL_LN + lane : FL_WD + min(lane - 32, 27)), tag DBG_PASS(2));
        LBAR();
        touch(lds0, A.ws + WS_WDF + (size_t)cb2 * 4 * 57344 + (size_t)((ix & 3) * 448 + min(tid, 447)) * 128);
        cin.u = ld_pub16(rs, WS_XNP + (dp * SL + tid * 4) * 4);
        ST(5);
        conv_uimage(cin, smem);
        conv_kimage(kix, smem);
        LBAR();
    }
    ST(6);
    f32x16 acc0, acc1;
    conv_mma(wave, smem, acc0, acc1);
    conv_handover(wave, smem, acc0, acc1);
    ST(7);
    const int ft = tid >> 3, fc = tid & 7;
    const u32x4 xv = *(const u32x4*)(A.x + (size_t)(rb2 * 64 + ft) * DM + cb2 * 64 + fc * 8);
    if (wave >= 4) {
        const int kw = wave - 4;
        const unsigned hb = hf_tile(rb2 * 4) + kw * 14 * 1024 + lane * 16;
        const unsigned wb = WS_WDF + ((cb2 * 4) * 56 + kw * 14) * 1024 + lane * 16;
        team_wait_flags<2>(wave == 4, lds0 + CTL_OFF + 24, fl + FL_H + (rb2 >> 1) * 16 + min(lane & 15, 13), tag DBG_PASS(3));
        ST(8);
        f32x4 acc[4][4];
#pragma unroll
        for (int i = 0; i < 4; ++i)
#pragma unroll
            for (int j = 0; j < 4; ++j) acc[i][j] = f32x4{0, 0, 0, 0};
        constexpr int PD = G2PD;
        s16x8 Af[PD][4], Bf[PD][4];
        auto lda = [&](int kt, int i) { Af[kt % PD][i] = __builtin_bit_cast(s16x8, ld_pub16(rs, wb + kt * 1024, i * 57344)); };
        auto ldb = [&](int kt, int i) { Bf[kt % PD][i] = __builtin_bit_cast(s16x8, ld_pub16(rs, hb + kt * 1024, i * 57344)); };
#pragma unroll
        for (int kt = 0; kt < PD - 1; ++kt) {
#pragma unroll
            for (int i = 0; i < 4; ++i) { lda(kt, i); ldb(kt, i); }
            SB();
        }
#pragma unroll
        for (int kt = 0; kt < 14; ++kt) {
            const bool ld = kt + PD - 1 < 14;
#pragma unroll
            for (int i = 0; i < 4; ++i) {
#pragma unroll
                for (int j = 0; j < 4; ++j) {
                    acc[i][j] = mfma16(Af[kt % PD][i], Bf[kt % PD][j], acc[i][j]);
                    if (ld && j == 0) lda(kt + PD - 1, i);
                    if (ld && j == 2) ldb(kt + PD - 1, i);
                    SB();
                }
            }
        }
        ST(10);
        const int m = lane & 15, g = lane >> 4;
#pragma unroll
        for (int i = 0; i < 4; ++i)
#pragma unroll
            for (int j = 0; j < 4; ++j) *(f32x4*)(smem + G2_P + ((kw * 4 + i) * 4 + j) * 1024 + (16 * g + (m ^ (4 * i + g))) * 16) = acc[i][j];
    } else {
        conv_finish(wave, dp, A, cin.ds, smem, acc0, acc1);
        ST(8);
        wait_vm<0>();
        unsigned old = 0;
        if (lane == 0) old = __hip_atomic_fetch_add((LAS unsigned*)LP(lds0 + CTL_OFF), 1u, __ATOMIC_RELAXED, __HIP_MEMORY_SCOPE_WORKGROUP);
        if (lane == 0 && old == 3) st_flag(fl + FL_S + dp, tag);
        ST(9);
        if (wave == 0) { __builtin_amdgcn_s_sleep(SWAIT0); }
        team_wait_flags<SWAIT1>(wave == 0, lds0 + CTL_OFF + 28, fl + FL_S + cb2 * 32 + (lane & 31), tag DBG_PASS(4));
        const int t2 = tid;
#pragma unroll
        for (int i = 0; i < 2; ++i) {
            const int d = (t2 >> 3) + 32 * i;
            const u32x4 sv = ld_pub16(rs, WS_SP + ((cb2 * 64 + d) * SL + rb2 * 64 + (t2 & 7) * 8) * 2);
            *(u32x4*)(smem + G2_S + d * 128 + (((t2 & 7) ^ ((d >> 3) & 7)) << 4)) = sv;
        }
        ST(10);
    }
    LBAR();
    ST(11);
    {
        const int mt = ft >> 4, m = ft & 15, dt = fc >> 1, g0 = 2 * (fc & 1);
        f32x4 y0 = {0, 0, 0, 0}, y1 = {0, 0, 0, 0};
#pragma unroll
        for (int s = 0; s < 4; ++s) {
            const char* base = smem + G2_P + ((s * 4 + dt) * 4 + mt) * 1024;
            y0 += *(const f32x4*)(base + (16 * g0 + (m ^ (4 * dt + g0))) * 16);
            y1 += *(const f32x4*)(base + (16 * (g0 + 1) + (m ^ (4 * dt + g0 + 1))) * 16);
        }
        const u16* S = (const u16*)(smem + G2_S + (8 * fc) * 128 + (((ft >> 3) ^ fc) << 4)) + (ft & 7);
        unsigned o[4];
#pragma unroll
        for (int j = 0; j < 4; ++j) {
            const f32x2 f = {rbf(j < 2 ? y0[2 * j] : y1[2 * j - 4]), rbf(j < 2 ? y0[2 * j + 1] : y1[2 * j - 3])};
            const f32x2 sv = {bf2f(S[(2 * j) * 64]), bf2f(S[(2 * j + 1) * 64])};
            const f32x2 gg = {rbf(f[0] * sigm(f[0])), rbf(f[1] * sigm(f[1]))};
            const f32x2 sg = sv * gg;
            o[j] = pk2(bflo(xv[j]) + rbf(sg[0]), bfhi(xv[j]) + rbf(sg[1]));
        }
        *(u32x4*)(A.out + (size_t)(rb2 * 64 + ft) * DM + cb2 * 64 + fc * 8) = u32x4{o[0], o[1], o[2], o[3]};
    }
    ST(12);
    if (b == 0) {
#pragma unroll 1
        for (int i = 0; i < 4; ++i) wait_flags(fl + FL_ARR + (wave * 4 + i) * 64 + lane, tag DBG_PASS(5));
        LBAR();
        if (tid == 0) st_flag(fl + FL_EPOCH, tag);
    }
    ST(15); ST_FLUSH(A.ws, 0);
}

extern "C" void kernel_launch(void* const* d_in, const int* in_sizes, int n_in, void* d_out, int out_size, void* d_ws, size_t ws_size,
                              hipStream_t stream) {
    Args A;
    A.x = (const u16*)d_in[0]; A.Are = (const u16*)d_in[1]; A.Aim = (const u16*)d_in[2]; A.Bre = (const u16*)d_in[3];
    A.Bim = (const u16*)d_in[4]; A.Cre = (const u16*)d_in[5]; A.Cim = (const u16*)d_in[6]; A.logdt = (const u16*)d_in[7];
    A.dskip = (const u16*)d_in[8]; A.wg = (const u16*)d_in[10]; A.wu = (const u16*)d_in[11]; A.wd = (const u16*)d_in[12];
    A.out = (u16*)d_out; A.ws = (char*)d_ws;
    (void)hipFuncSetAttribute((const void*)k_fused, hipFuncAttributeMaxDynamicSharedMemorySize, FUSED_LDS);
    k_fused<<<256, 512, FUSED_LDS, stream>>>(A);
}
```

```cpp
#define FUSED
#include <hip/hip_runtime.h>
#include <stdint.h>
#include <type_traits>

#define GAS __attribute__((address_space(1)))
#define LAS __attribute__((address_space(3)))
typedef __attribute__((ext_vector_type(4))) short s16x4;
typedef __attribute__((ext_vector_type(8))) short s16x8;
typedef __attribute__((ext_vector_type(8))) __bf16 bf16x8;
typedef __attribute__((ext_vector_type(2))) __bf16 bf16x2;
typedef __attribute__((ext_vector_type(2))) float f32x2;
typedef __attribute__((ext_vector_type(4))) float f32x4;
typedef __attribute__((ext_vector_type(16))) float f32x16;
typedef __attribute__((ext_vector_type(4))) unsigned u32x4;
typedef __attribute__((ext_vector_type(2))) unsigned u32x2;
typedef unsigned short u16;

constexpr int SL = 2048, DM = 512, NS = 128, FF = 1792;

#define SB() __builtin_amdgcn_sched_barrier(0)
#define BAR() do { __builtin_amdgcn_s_barrier(); asm volatile("" ::: "memory"); } while (0)
#define LP(a) ((LAS char*)(size_t)(unsigned)(a))
template <int N> __device__ __forceinline__ void wait_vm() { asm volatile("s_waitcnt vmcnt(%0)" :: "n"(N) : "memory"); }
__device__ __forceinline__ void wait_lgkm0() { asm volatile("s_waitcnt lgkmcnt(0)" ::: "memory"); }
#define LBAR() do { wait_lgkm0(); BAR(); } while (0)

__device__ __forceinline__ float bf2f(u16 v) { return __uint_as_float(((unsigned)v) << 16); }
__device__ __forceinline__ float bflo(unsigned v) { return __uint_as_float(v << 16); }
__device__ __forceinline__ float bfhi(unsigned v) { return __uint_as_float(v & 0xffff0000u); }
__device__ __forceinline__ unsigned pk2(float a, float b) {
    f32x2 z = {a, b};
    return __builtin_bit_cast(unsigned, __builtin_convertvector(z, bf16x2));
}
__device__ __forceinline__ float rbf(float a) { return bflo(pk2(a, 0.f)); }
__device__ __forceinline__ float sigm(float x) { return __builtin_amdgcn_rcpf(1.f + __builtin_amdgcn_exp2f(x * -1.44269504088896341f)); }
__device__ __forceinline__ f32x4 mfma16(s16x8 a, s16x8 b, f32x4 c) {
    return __builtin_amdgcn_mfma_f32_16x16x32_bf16(__builtin_bit_cast(bf16x8, a), __builtin_bit_cast(bf16x8, b), c, 0, 0, 0);
}
__device__ __forceinline__ f32x16 mfma32(s16x8 a, s16x8 b, f32x16 c) {
    return __builtin_amdgcn_mfma_f32_32x32x16_bf16(__builtin_bit_cast(bf16x8, a), __builtin_bit_cast(bf16x8, b), c, 0, 0, 0);
}
__device__ __forceinline__ void set_m0(unsigned lds) { asm volatile("s_mov_b32 m0, %0" :: "s"(lds)); }
template <int I> __device__ __forceinline__ void glds_o(const char* g, unsigned off) {
    asm volatile("global_load_lds_dwordx4 %1, %0 offset:%2" :: "s"(g), "v"(off), "n"(I * 1024));
}

#define ST_DECL
#define ST(i)
#define ST_CLK(i)
#define ST_FLUSH(wsbase, kid)
#define ST_ARG
#define ST_PASS
constexpr size_t PG = 2u << 20;
constexpr size_t WS_FLAG  = 0;
constexpr size_t WS_STATS = 32768;
constexpr size_t WS_XNP   = 1 * PG + 4096;
constexpr size_t WS_HF    = 3 * PG + 4096;
constexpr size_t WS_WDF   = 7 * PG + 4096;
constexpr size_t WS_SP    = 8 * PG + 4096;
constexpr size_t WS_END   = 10 * PG;
__host__ __device__ constexpr unsigned hf_tile(int mt) { return (unsigned)WS_HF + (unsigned)(mt >> 5) * (unsigned)PG + (unsigned)(mt & 31) * 57344u; }
constexpr int FL_EPOCH = 0, FL_LN = 32, FL_CS = 64, FL_WD = 96, FL_H = 256  , FL_S = 512  , FL_ARR = 768  ;

struct Args {
    const u16 *x, *Are, *Aim, *Bre, *Bim, *Cre, *Cim, *logdt, *dskip, *wg, *wu, *wd;
    u16* out;
    char* ws;
};
typedef __amdgpu_buffer_rsrc_t rsrc_t;
__device__ __forceinline__ rsrc_t ws_rsrc(const Args& A) { return __builtin_amdgcn_make_buffer_rsrc(A.ws, 0, (unsigned)WS_END, 0x00020000); }
constexpr int AUX_PUB = 16;
__device__ __forceinline__ void st_pub16(rsrc_t rs, unsigned off, u32x4 v) { __builtin_amdgcn_raw_buffer_store_b128(v, rs, off, 0, AUX_PUB); }
__device__ __forceinline__ void st_pub8(rsrc_t rs, unsigned off, u32x2 v) { __builtin_amdgcn_raw_buffer_store_b64(v, rs, off, 0, AUX_PUB); }
__device__ __forceinline__ u32x4 ld_pub16(rsrc_t rs, unsigned off, unsigned soff = 0) { return __builtin_amdgcn_raw_buffer_load_b128(rs, off, soff, AUX_PUB); }
__device__ __forceinline__ u32x2 ld_pub8(rsrc_t rs, unsigned off) { return __builtin_amdgcn_raw_buffer_load_b64(rs, off, 0, AUX_PUB); }

template <int CTRL> __device__ __forceinline__ float dpp_f(float x) {
    return __builtin_bit_cast(float, __builtin_amdgcn_mov_dpp(__builtin_bit_cast(int, x), CTRL, 0xf, 0xf, true));
}
__device__ __forceinline__ float row16_sum(float x) {
    x += dpp_f<0x128>(x); x += dpp_f<0x124>(x); x += dpp_f<0x122>(x); x += dpp_f<0x121>(x);
    return x;
}
__device__ __forceinline__ float xrow_sum(float x) {
    auto s = __builtin_amdgcn_permlane16_swap(__float_as_uint(x), __float_as_uint(x), false, false);
    x = __uint_as_float(s[0]) + __uint_as_float(s[1]);
    auto t = __builtin_amdgcn_permlane32_swap(__float_as_uint(x), __float_as_uint(x), false, false);
    return __uint_as_float(t[0]) + __uint_as_float(t[1]);
}
struct AuxX { u32x4 x[8]; };
struct AuxW { u32x4 w[16]; };
struct AuxD { u32x4 d[8]; };
__device__ __forceinline__ void aux_load_x(int a, const Args& A, AuxX& in) {
    const int tid = threadIdx.x, lane = tid & 63, wave = tid >> 6;
#pragma unroll
    for (int i = 0; i < 8; ++i)
        in.x[i] = *(const u32x4*)(A.x + (size_t)(a * 64 + wave * 8 + (i >> 2) * 4 + (lane >> 4)) * DM + ((i & 3) * 16 + (lane & 15)) * 8);
}
__device__ __forceinline__ void aux_load_w(int a, const Args& A, AuxW& in) {
    const int tid = threadIdx.x, c = tid & 7, r = tid >> 3;
#pragma unroll
    for (int i = 0; i < 16; ++i) in.w[i] = *(const u32x4*)(((i >> 3) ? A.wu : A.wg) + (size_t)(r + (i & 7) * 64) * FF + a * 64 + c * 8);
}
__device__ __forceinline__ void aux_load_d(int a, const Args& A, AuxD& in) {
    const int tid = threadIdx.x;
#pragma unroll
    for (int i = 0; i < 8; ++i) { const int cc = i * 512 + tid; in.d[i] = *(const u32x4*)(A.wd + (size_t)(a * 64 + (cc >> 6)) * DM + (cc & 63) * 8); }
}
__device__ __forceinline__ float dot2bf(unsigned a, unsigned b, float c) {
    return __builtin_amdgcn_fdot2_f32_bf16(__builtin_bit_cast(bf16x2, a), __builtin_bit_cast(bf16x2, b), c, false);
}
__device__ __forceinline__ void ln_part(int a, const Args& A, const AuxX& in, char* smem) {
    const int tid = threadIdx.x, lane = tid & 63, wave = tid >> 6;
    const rsrc_t rs = ws_rsrc(A);
#pragma unroll
    for (int ps = 0; ps < 2; ++ps) {
        float s0 = 0.f, s1 = 0.f, q0 = 0.f, q1 = 0.f;
#pragma unroll
        for (int i = 0; i < 4; ++i)
#pragma unroll
            for (int j = 0; j < 4; j += 2) {
                const unsigned u0 = in.x[4 * ps + i][j], u1 = in.x[4 * ps + i][j + 1];
                s0 = dot2bf(u0, 0x3f803f80u, s0); s1 = dot2bf(u1, 0x3f803f80u, s1);
                q0 = dot2bf(u0, u0, q0); q1 = dot2bf(u1, u1, q1);
            }
        const float mean = row16_sum(s0 + s1) * (1.f / DM);
        const float var = rbf(fmaxf(row16_sum(q0 + q1) * (1.f / DM) - mean * mean, 0.f));
        const float den = rbf(__builtin_sqrtf(rbf(var + 1e-5f)));
        const float mb = rbf(mean), rd = 1.f / den, nm = -mb * rd;
        const int r = wave * 8 + ps * 4 + (lane >> 4);
        if ((lane & 15) == 0) st_pub8(rs, WS_STATS + (a * 64 + r) * 8, u32x2{__float_as_uint(mb), __float_as_uint(rd)});
#pragma unroll
        for (int i = 0; i < 4; ++i) {
            u32x4 o;
#pragma unroll
            for (int j = 0; j < 4; ++j) {
                const unsigned u = in.x[4 * ps + i][j];
                const f32x2 v = __builtin_elementwise_fma(f32x2{bflo(u), bfhi(u)}, f32x2{rd, rd}, f32x2{nm, nm});
                o[j] = pk2(v[0], v[1]);
            }
            *(u32x4*)(smem + r * 1024 + (((i * 16 + (lane & 15)) ^ ((r >> 2) & 7)) << 4)) = o;
        }
    }
    LBAR();
    const unsigned* T = (const unsigned*)smem;
#pragma unroll
    for (int it = 0; it < 8; ++it) {
        const int dp = it * 32 + wave * 4 + (lane >> 4), tq = lane & 15;
        u32x4 o;
#pragma unroll
        for (int j = 0; j < 4; ++j) o[j] = T[(4 * tq + j) * 256 + (((dp >> 2) ^ (tq & 7)) << 2) + (dp & 3)];
        st_pub16(rs, WS_XNP + (dp * SL + a * 64 + 4 * tq) * 4, o);
    }
}
__device__ __forceinline__ void wd_part(int a, const Args& A, const AuxD& in, char* smem) {
    const int tid = threadIdx.x;
#pragma unroll
    for (int i = 0; i < 8; ++i) {
        const int c = i * 512 + tid, r = c >> 6, cc = c & 63;
        *(u32x4*)(smem + r * 1024 + ((((cc >> 1) ^ (r & 7)) << 1) | (cc & 1)) * 16) = in.d[i];
    }
    LBAR();
    const rsrc_t rs = ws_rsrc(A);
    const int lane = tid & 63, wave = tid >> 6, g = lane >> 4, q = (lane & 15) >> 2, p = lane & 3;
    const unsigned lds0 = (unsigned)(size_t)(LAS char*)smem;
#pragma unroll
    for (int i = 0; i < 8; ++i) {
        const int f = i * 8 + wave, kk = f >> 5, dt = f & 31;
        const int r0 = 32 * kk + 4 * g + q, r1 = r0 + 16;
        const s16x4 lo = __builtin_amdgcn_ds_read_tr16_b64_v4i16((LAS s16x4*)LP(lds0 + r0 * 1024 + ((dt ^ (r0 & 7)) << 5) + p * 8));
        const s16x4 hi = __builtin_amdgcn_ds_read_tr16_b64_v4i16((LAS s16x4*)LP(lds0 + r1 * 1024 + ((dt ^ (r1 & 7)) << 5) + p * 8));
        st_pub16(rs, WS_WDF + ((dt * 56 + 2 * a + kk) * 64 + lane) * 16, __builtin_bit_cast(u32x4, __builtin_shufflevector(lo, hi, 0, 1, 2, 3, 4, 5, 6, 7)));
    }
}

typedef f32x2 cpx;
__device__ __forceinline__ cpx cmul(cpx a, cpx b) {
    cpx r;
    asm("v_pk_mul_f32 %0, %1, %2 op_sel:[0,0] op_sel_hi:[0,1]\n\t"
        "v_pk_fma_f32 %0, %1, %2, %0 op_sel:[1,1,0] op_sel_hi:[1,0,1] neg_lo:[1,0,0]" : "=&v"(r) : "v"(a), "v"(b));
    return r;
}
__device__ __forceinline__ cpx cexp_p(float are2, float airev, float p) {
    const float er = __builtin_amdgcn_exp2f(are2 * p);
    const float rv = __builtin_amdgcn_fractf(airev * p);
    return cpx{er * __builtin_amdgcn_cosf(rv), er * __builtin_amdgcn_sinf(rv)};
}
struct KgIn { u16 are, aim, bre, bim, cre, cim, ldt; };
__device__ __forceinline__ KgIn kg_load(int dp, const Args& A) {
    const int tid = threadIdx.x, lane = tid & 63, wave = tid >> 6;
    const int d = dp * 2 + ((wave >> 1) & 1), i = d * NS + (wave & 1) * 64 + lane;
    KgIn k;
    k.are = A.Are[i]; k.aim = A.Aim[i]; k.bre = A.Bre[i]; k.bim = A.Bim[i]; k.cre = A.Cre[i]; k.cim = A.Cim[i]; k.ldt = A.logdt[d];
    return k;
}
constexpr int KG_TAB = 16 * 81 * 16;
constexpr int KC_OFF = 88 * 1024;
__device__ __forceinline__ void cmul2(cpx& r0, cpx a0, cpx b0, cpx& r1, cpx a1, cpx b1) {
    asm("v_pk_mul_f32 %0, %2, %3 op_sel:[0,0] op_sel_hi:[0,1]\n\t"
        "v_pk_mul_f32 %1, %4, %5 op_sel:[0,0] op_sel_hi:[0,1]\n\t"
        "v_pk_fma_f32 %0, %2, %3, %0 op_sel:[1,1,0] op_sel_hi:[1,0,1] neg_lo:[1,0,0]\n\t"
        "v_pk_fma_f32 %1, %4, %5, %1 op_sel:[1,1,0] op_sel_hi:[1,0,1] neg_lo:[1,0,0]"
        : "=&v"(r0), "=&v"(r1) : "v"(a0), "v"(b0), "v"(a1), "v"(b1));
}
struct KgVals { unsigned v[41]; };
template <typename Hook>
__device__ __forceinline__ void kgen_values(const KgIn& in, KgVals& o, Hook hook) {
    const int wave = __builtin_amdgcn_readfirstlane(threadIdx.x >> 6);
    const float dt = rbf(__builtin_amdgcn_exp2f(bf2f(in.ldt) * 1.44269504088896341f));
    const float are2 = dt * bf2f(in.are) * 1.44269504088896341f, airev = dt * bf2f(in.aim) * 0.15915494309189535f;
    auto put = [&](int i, cpx v) { o.v[i] = pk2(v[0], v[1]); };
    if (wave < 4) {
        cpx P[16];
        P[0] = cpx{1.f, 0.f};
        P[1] = cexp_p(are2, -airev, 1.f);
        hook(0);
        P[2] = cmul(P[1], P[1]);
        cmul2(P[3], P[2], P[1], P[4], P[2], P[2]);
        cmul2(P[5], P[4], P[1], P[6], P[3], P[3]);
        cmul2(P[7], P[6], P[1], P[8], P[4], P[4]);
        cmul2(P[9], P[8], P[1], P[10], P[5], P[5]);
        cmul2(P[11], P[10], P[1], P[12], P[6], P[6]);
        cmul2(P[13], P[12], P[1], P[14], P[7], P[7]);
        P[15] = cmul(P[14], P[1]);
        hook(1);
#pragma unroll
        for (int b = 0; b < 16; ++b) put(b, P[b]);
#pragma unroll
        for (int s = 0; s < 3; ++s) {
            hook(2 + s);
#pragma unroll
            for (int i = 0; i < 8; i += 2) {
                cpx a0 = P[8 + i], a1 = P[9 + i];
                cmul2(P[8 + i], a0, a0, P[9 + i], a1, a1);
                put(16 + 8 * s + i, P[8 + i]); put(17 + 8 * s + i, P[9 + i]);
            }
        }
        o.v[40] = 0;
    } else {
        const cpx dB = {dt * bf2f(in.bre), dt * bf2f(in.bim)};
        const cpx w = cmul(cpx{bf2f(in.cre), bf2f(in.cim)}, dB);
        const cpx z16 = cexp_p(are2, airev, 16.f), z128 = cexp_p(are2, airev, 128.f);
        cpx z32, z64, z256, z512, p0, p1;
        hook(0);
        cmul2(z32, z16, z16, z256, z128, z128);
        cmul2(z64, z32, z32, z512, z256, z256);
        const cpx z1024 = cmul(z512, z512);
        p0 = w; p1 = cmul(w, z16);
        hook(1);
#pragma unroll
        for (int a = 0; a < 16; a += 2) {
            put(a, p0); put(1 + a, p1);
            if (a < 14) { cpx q0 = p0, q1 = p1; cmul2(p0, q0, z32, p1, q1, z32); }
            if (a == 6) hook(2);
        }
        cmul2(p0, w, z256, p1, w, z512);
        hook(3);
#pragma unroll
        for (int a = 0; a < 8; ++a) {
            put(16 + a, p0); put(24 + a, p1);
            if (a < 7) { cpx q0 = p0, q1 = p1; cmul2(p0, q0, z32, p1, q1, z64); }
        }
        p0 = cmul(w, z1024); p1 = cmul(p0, z128);
        hook(4);
#pragma unroll
        for (int a = 0; a < 8; a += 2) {
            put(32 + a, p0); put(33 + a, p1);
            cpx q0 = p0, q1 = p1; cmul2(p0, q0, z256, p1, q1, z256);
        }
        put(40, p0);
    }
#pragma unroll
    for (int i = 0; i < 41; ++i) asm volatile("" : "+v"(o.v[i]));
}
__device__ __forceinline__ void kgen_tables(const KgVals& in, char* smem) {
    const int tid = threadIdx.x, lane = tid & 63, wave = __builtin_amdgcn_readfirstlane(tid >> 6);
    unsigned* tab = (unsigned*)(smem + (wave & 3) * KG_TAB) + (lane >> 2) * (81 * 4) + (lane & 3) + (wave >> 2) * (40 * 4);
#pragma unroll
    for (int i = 0; i < 40; ++i) tab[i * 4] = in.v[i];
    if (wave >= 4) tab[40 * 4] = in.v[40];
}
__device__ __forceinline__ void kgen_contract(int ch, int s, char* smem) {
    const int lane = threadIdx.x & 63, b = lane & 15, g = lane >> 4;
    const int ra = s == 0 ? 40 + b : (s == 3 ? 72 + min(b, 8) : 48 + 8 * s + (b & 7));
    const int sh = min(s, b == 0 ? 3 : __builtin_clz((unsigned)b) - 28), bb = b << sh, e = s - sh;
    const int rb = e > 0 ? 16 + 8 * (e - 1) + (bb - 8) : bb;
    const char* ta = smem + ch * 2 * KG_TAB + (g * 81 + ra) * 16;
    const char* tb = smem + ch * 2 * KG_TAB + (g * 81 + rb) * 16;
    f32x4 acc = {0, 0, 0, 0};
#pragma unroll
    for (int k = 0; k < 8; ++k)
        acc = mfma16(*(const s16x8*)(ta + (k >> 2) * KG_TAB + (k & 3) * (4 * 81 * 16)), *(const s16x8*)(tb + (k >> 2) * KG_TAB + (k & 3) * (4 * 81 * 16)), acc);
    float* kc = (float*)(smem + KC_OFF) + ch * 656 + (s ? 128 * (s + 1) : 0) + 64 * g + b;
    if (s == 0 || g < 2) { kc[0] = acc[0]; kc[16] = acc[1]; kc[32] = acc[2]; kc[48] = acc[3]; }
    else if (s == 3 && g == 2) kc[0] = acc[0];
}

constexpr int KR_C = 2087;
constexpr int CV_KSTR = 4416;
constexpr int CV_KB = 4 * CV_KSTR;
constexpr int CV_UB = 96 * 80;
constexpr int CV_U = 36 * 1024;
constexpr int CV_X = 52 * 1024;
constexpr int G2_S = 68 * 1024;
constexpr int G2_P = 94 * 1024;
constexpr int CTL_OFF = 158 * 1024;
struct CvIn { u32x4 u[2]; u16 ds; };
__device__ __forceinline__ void conv_uimage(const CvIn& in, char* smem) {
    const int tid = threadIdx.x;
#pragma unroll
    for (int k = 0; k < 2; ++k) {
        const int idx = 1024 + 1024 * k + tid * 4, off = (idx >> 5) * 80 + (idx & 31) * 2;
        *(u32x2*)(smem + CV_U + off) = u32x2{__builtin_amdgcn_perm(in.u[k][1], in.u[k][0], 0x05040100), __builtin_amdgcn_perm(in.u[k][3], in.u[k][2], 0x05040100)};
        *(u32x2*)(smem + CV_U + CV_UB + off) = u32x2{__builtin_amdgcn_perm(in.u[k][1], in.u[k][0], 0x07060302), __builtin_amdgcn_perm(in.u[k][3], in.u[k][2], 0x07060302)};
    }
    const int ch = tid >> 7, zi = (tid & 127) * 8;
    *(u32x4*)(smem + CV_U + ch * CV_UB + (zi >> 5) * 80 + (zi & 31) * 2) = u32x4{0, 0, 0, 0};
}
struct KIdx { unsigned p[6]; };
__device__ __forceinline__ void conv_kindex(KIdx& o) {
    const int i = threadIdx.x & 255, top = 2050 - 8 * i;
#pragma unroll
    for (int e = 0; e < 12; e += 2) {
        const int l0 = min(top - e, SL - 1), l1 = min(max(top - e - 1, 0), SL - 1);
        const unsigned bb = pk2((float)l0, (float)l1);
        const unsigned i0 = l0 < 128 ? l0 : (bb & 0xffffu) - 17024u, i1 = l1 < 128 ? l1 : (bb >> 16) - 17024u;
        o.p[e >> 1] = (i0 << 2) | (i1 << 18);
        asm volatile("" : "+v"(o.p[e >> 1]));
    }
}
__device__ __forceinline__ void conv_kimage(int ch, const KIdx& ix, char* smem) {
    const int i = threadIdx.x & 255;
    const char* kc = smem + KC_OFF + ch * 656 * 4;
    float v[11];
#pragma unroll
    for (int e = 0; e < 12; e += 2) {
        v[e] = *(const float*)(kc + (ix.p[e >> 1] & 0xffffu));
        if (e + 1 < 11) v[e + 1] = *(const float*)(kc + (ix.p[e >> 1] >> 16));
    }
    if (i == 0) { v[0] = 0.f; v[1] = 0.f; v[2] = 0.f; }
    unsigned ev[5], od[5];
#pragma unroll
    for (int k = 0; k < 5; ++k) { ev[k] = pk2(v[2 * k], v[2 * k + 1]); od[k] = pk2(v[2 * k + 1], v[2 * k + 2]); }
    char* dst = smem + ch * CV_KB + (i + 5) * 16;
    *(u32x4*)(dst) = u32x4{od[1], od[2], od[3], od[4]};
    *(u32x4*)(dst + CV_KSTR) = u32x4{ev[1], ev[2], ev[3], ev[4]};
    *(u32x4*)(dst + 2 * CV_KSTR) = u32x4{od[0], od[1], od[2], od[3]};
    *(u32x4*)(dst + 3 * CV_KSTR) = u32x4{ev[0], ev[1], ev[2], ev[3]};
    if (i == 255) {
        char* e = smem + ch * CV_KB + 261 * 16;
        *(u32x4*)(e) = u32x4{0, 0, 0, 0};
        *(u32x4*)(e + CV_KSTR) = u32x4{pk2(v[10], 0.f), 0, 0, 0};
        *(u32x4*)(e + 2 * CV_KSTR) = u32x4{od[4], 0, 0, 0};
        *(u32x4*)(e + 3 * CV_KSTR) = u32x4{ev[4], pk2(v[10], 0.f), 0, 0};
    }
    if (i < 32) *(u32x4*)(smem + ch * CV_KB + (i >> 3) * CV_KSTR + ((i & 7) < 5 ? (i & 7) : 257 + (i & 7)) * 16) = u32x4{0, 0, 0, 0};
}
template <int Q0, int Q1, int NTILE>
__device__ __forceinline__ void conv_span(unsigned fa, unsigned ua, f32x16& acc0, f32x16& acc1) {
    constexpr int N = Q1 - Q0 + 1, PD = NTILE == 2 ? 4 : 6;
    const unsigned fb = fa - 32 * Q1, ub = ua - ((Q1 + 1) / 2) * 80;
    unsigned fb2 = fb + 8;
    asm volatile("" : "+v"(fb2));
    u32x2 flo[PD], fhi[PD];
    s16x8 b0[PD], b1[PD];
    auto ld = [&](int q, int sl) {
        const int fo = 32 * (Q1 - q);
        flo[sl] = *(const LAS u32x2*)(LP(fb) + fo); fhi[sl] = *(const LAS u32x2*)(LP(fb2) + fo);
        const int qq = q & 1, blk = (q + qq) / 2;
        const int uo = ((Q1 + 1) / 2 - blk) * 80 + 32 * qq;
        if (NTILE == 2) b0[sl] = *(const LAS s16x8*)(LP(ub) + uo);
        b1[sl] = *(const LAS s16x8*)(LP(ub) + uo + 32 * 80);
    };
#pragma unroll
    for (int i = 0; i < PD; ++i) if (i < N) ld(Q0 + i, i);
    SB();
#pragma unroll
    for (int i = 0; i < N; ++i) {
        const int sl = i % PD;
        const s16x8 F = __builtin_bit_cast(s16x8, u32x4{flo[sl][0], flo[sl][1], fhi[sl][0], fhi[sl][1]});
        const s16x8 x0 = b0[sl], x1 = b1[sl];
        if (NTILE == 2) acc0 = mfma32(F, x0, acc0);
        acc1 = mfma32(F, x1, acc1);
        if (i + PD < N) ld(Q0 + i + PD, sl);
        SB();
    }
}
__device__ __forceinline__ void conv_mma(int wave, char* smem, f32x16& acc0, f32x16& acc1) {
    const int lane = threadIdx.x & 63;
    const unsigned lds0 = (unsigned)(size_t)(LAS char*)smem;
    const int ch = wave >> 1;
    const int r = lane & 31, h = lane >> 5, S = (r + 1) & 3;
    const unsigned fa = lds0 + ch * CV_KB + S * CV_KSTR + (KR_C - r + 8 * h + S) * 2;
    const unsigned ua = lds0 + CV_U + ch * CV_UB + (32 + r) * 80 + 16 * h;
#pragma unroll
    for (int i = 0; i < 16; ++i) { acc0[i] = 0.f; acc1[i] = 0.f; }
    if ((wave & 1) == 0) conv_span<-1, 47, 2>(fa, ua, acc0, acc1);
    else { conv_span<48, 64, 2>(fa, ua, acc0, acc1); conv_span<65, 128, 1>(fa, ua, acc0, acc1); }
}
__device__ __forceinline__ unsigned lds_ld(unsigned addr) { return __hip_atomic_load((LAS unsigned*)LP(addr), __ATOMIC_RELAXED, __HIP_MEMORY_SCOPE_WORKGROUP); }
__device__ __forceinline__ void lds_st(unsigned addr, unsigned v) { __hip_atomic_store((LAS unsigned*)LP(addr), v, __ATOMIC_RELAXED, __HIP_MEMORY_SCOPE_WORKGROUP); }
__device__ __forceinline__ unsigned lds_add(unsigned addr) { return __hip_atomic_fetch_add((LAS unsigned*)LP(addr), 1u, __ATOMIC_RELAXED, __HIP_MEMORY_SCOPE_WORKGROUP); }
__device__ __forceinline__ void team_bar(unsigned word, unsigned target) {
    wait_lgkm0();
    if ((threadIdx.x & 63) == 0) lds_add(word);
    while (__builtin_amdgcn_readfirstlane(lds_ld(word)) < target) __builtin_amdgcn_s_sleep(0);
    asm volatile("" ::: "memory");
}
__device__ __forceinline__ void conv_finish(int wave, int dp, const Args& A, u16 dsk, char* smem, const f32x16& acc0, const f32x16& acc1) {
    const int lane = threadIdx.x & 63;
    const unsigned lds0 = (unsigned)(size_t)(LAS char*)smem;
    const int ch = wave >> 1, o = wave & 1, r = lane & 31, h = lane >> 5;
    f32x16 y;
    {
        f32x4* X = (f32x4*)(smem + CV_X + wave * 4096) + lane;
        if (o == 0) {
#pragma unroll
            for (int i = 0; i < 4; ++i) X[i * 64] = f32x4{acc1[4 * i], acc1[4 * i + 1], acc1[4 * i + 2], acc1[4 * i + 3]};
            y = acc0;
        } else {
#pragma unroll
            for (int i = 0; i < 4; ++i) X[i * 64] = f32x4{acc0[4 * i], acc0[4 * i + 1], acc0[4 * i + 2], acc0[4 * i + 3]};
            y = acc1;
        }
        team_bar(lds0 + CTL_OFF + 8 + ch * 4, 2);
        const f32x4* Y = (const f32x4*)(smem + CV_X + (wave ^ 1) * 4096) + lane;
#pragma unroll
        for (int i = 0; i < 4; ++i) {
            const f32x4 p = Y[i * 64];
#pragma unroll
            for (int e = 0; e < 4; ++e) y[4 * i + e] += p[e];
        }
    }
    const float ds = bf2f(dsk);
    char* S = smem + CV_X + (wave ^ 1) * 4096;
    const char* U = smem + CV_U + ch * CV_UB + (32 + 32 * o + r) * 80 + 8 * h;
#pragma unroll
    for (int i = 0; i < 4; ++i) {
        const u32x2 uu = *(const u32x2*)(U + 16 * i);
        const f32x2 y0 = f32x2{y[4 * i], y[4 * i + 1]} + f32x2{bflo(uu[0]), bfhi(uu[0])} * ds;
        const f32x2 y1 = f32x2{y[4 * i + 2], y[4 * i + 3]} + f32x2{bflo(uu[1]), bfhi(uu[1])} * ds;
        *(u32x2*)(S + r * 64 + ((i ^ ((r >> 1) & 3)) << 4) + h * 8) = u32x2{pk2(y0[0], y0[1]), pk2(y1[0], y1[1])};
    }
    wait_lgkm0();
    const rsrc_t rs = ws_rsrc(A);
#pragma unroll
    for (int i = 0; i < 2; ++i)
        st_pub16(rs, WS_SP + ((dp * 2 + ch) * SL + 1024 * o) * 2 + i * 1024 + lane * 16, *(const u32x4*)(S + i * 1024 + (lane >> 2) * 64 + (((lane & 3) ^ ((lane >> 3) & 3)) << 4)));
}
typedef unsigned long long u64;
__device__ __forceinline__ void st_flag(u64* p, u64 v) { __hip_atomic_store((GAS u64*)p, v, __ATOMIC_RELAXED, __HIP_MEMORY_SCOPE_AGENT); }
__device__ __forceinline__ u64 ld_flag(const u64* p) { return __hip_atomic_load((GAS u64*)p, __ATOMIC_RELAXED, __HIP_MEMORY_SCOPE_AGENT); }
#define DBG_TMO(code)
#define DBG_ARG
#define DBG_PASS(c)
template <int SLEEP = 4>
__device__ __forceinline__ void wait_flags(const u64* p, u64 tag DBG_ARG) {
    const u64 t0 = __builtin_amdgcn_s_memrealtime();
    while (!__all(ld_flag(p) == tag)) {
        if (__builtin_amdgcn_s_memrealtime() - t0 > 300000ull) { DBG_TMO(code); break; }
        __builtin_amdgcn_s_sleep(SLEEP);
    }
    asm volatile("" ::: "memory");
}
template <int SLEEP = 4>
__device__ __forceinline__ void team_wait_flags(bool poller, unsigned lds_word, const u64* p, u64 tag DBG_ARG) {
    if (poller) {
        wait_flags<SLEEP>(p, tag);
        if ((threadIdx.x & 63) == 0) lds_st(lds_word, 1u);
    } else {
        while (__builtin_amdgcn_readfirstlane(lds_ld(lds_word)) == 0) __builtin_amdgcn_s_sleep(4);
        asm volatile("" ::: "memory");
    }
}
__device__ __forceinline__ void poll_issue(const u64* p) { asm volatile("global_load_dwordx2 v[254:255], %0, off sc1" :: "v"(p) : "v254", "v255"); }
template <int SLEEP = 4>
__device__ __forceinline__ void poll_check(const u64* p, u64 tag DBG_ARG) {
    unsigned lo, hi;
    asm volatile("s_waitcnt vmcnt(0)\n\tv_mov_b32 %0, v254\n\tv_mov_b32 %1, v255" : "=v"(lo), "=v"(hi) :: "memory");
    const u64 v = ((u64)hi << 32) | lo;
    if (!__all(v == tag)) wait_flags<SLEEP>(p, tag);
}
template <int OFF> __device__ __forceinline__ void glds_dw(const void* p) {
    asm volatile("global_load_lds_dword %0, off offset:%1" :: "v"((const char*)p - OFF), "n"(OFF));
}
template <int OFF> __device__ __forceinline__ void glds_dw_sc1(const void* p) {
    asm volatile("global_load_lds_dword %0, off offset:%1 sc1" :: "v"((const char*)p - OFF), "n"(OFF));
}
constexpr int DUMMY_OFF = CTL_OFF + 512;
__device__ __forceinline__ void touch(unsigned lds0, const void* p) { set_m0(lds0 + DUMMY_OFF); asm volatile("s_nop 0"); glds_dw_sc1<0>(p); }
__device__ __forceinline__ unsigned kg_area(int wave) { return 32 * 1024 + wave * 2048; }
__device__ __forceinline__ void kg_issue(int dp, const Args& A, unsigned lds0) {
    const int tid = threadIdx.x, lane = tid & 63, wave = __builtin_amdgcn_readfirstlane(tid >> 6);
    const int d = dp * 2 + ((wave >> 1) & 1), i = d * NS + 2 * lane;
    set_m0(lds0 + kg_area(wave));
    asm volatile("s_nop 0");
    glds_dw<0>(A.Are + i); glds_dw<256>(A.Aim + i); glds_dw<512>(A.Bre + i); glds_dw<768>(A.Bim + i);
    glds_dw<1024>(A.Cre + i); glds_dw<1280>(A.Cim + i); glds_dw<1536>(A.logdt + (d & ~1));
}
constexpr int EPOCH_OFF = CTL_OFF + 256;
__device__ __forceinline__ void epoch_issue(const Args& A, unsigned lds0) {
    set_m0(lds0 + EPOCH_OFF);
    asm volatile("s_nop 0");
    glds_dw_sc1<0>((const char*)(A.ws + WS_FLAG) + FL_EPOCH * 8 + (threadIdx.x & 1) * 4);
}
__device__ __forceinline__ u64 epoch_read(const char* smem) { return *(const u64*)(smem + EPOCH_OFF) + 1; }
__device__ __forceinline__ KgIn kg_read(const char* smem) {
    const int tid = threadIdx.x, lane = tid & 63, wave = __builtin_amdgcn_readfirstlane(tid >> 6);
    const char* ar = smem + kg_area(wave);
    const u16* pm = (const u16*)ar + (wave & 1) * 64 + lane;
    KgIn r;
    r.are = pm[0]; r.aim = pm[128]; r.bre = pm[256]; r.bim = pm[384]; r.cre = pm[512]; r.cim = pm[640];
    r.ldt = ((const u16*)(ar + 1536))[(wave >> 1) & 1];
    wait_lgkm0();
    return r;
}

constexpr int XS = 16384, WSL = 16384, NXS = 3, NWS = 6;
constexpr int CS_OFF = CTL_OFF + 1024;
constexpr int FUSED_LDS = 160 * 1024;
#define SWAIT0 4
#define SWAIT1 16
#define CVPRIO 2
#define CSM 1
#define NO_CS 0
#define G2PD 4
#define TLBW 0
#define TLBW_CODE_PAGES 6
#define TLBW_BACK 0
#define G2MODE 0
#define G2PRIO 3
#define WTOUCH 0
#define G2SIG 10
#define PF 0

__global__ __launch_bounds__(512, 2) void k_fused(Args A_) {
    extern __shared__ __attribute__((aligned(16))) char smem[];
    const Args& A = A_;
    ST_DECL; ST(0);
    const int tid = threadIdx.x, lane = tid & 63, wave = __builtin_amdgcn_readfirstlane(tid >> 6);
    const int b = blockIdx.x, xcd = b & 7, ix = b >> 3;
    const int dp = b;
    const int rb2 = xcd * 4 + (ix & 3), cb2 = ix >> 2;
    u64* fl = (u64*)(A.ws + WS_FLAG);
    const rsrc_t rs = ws_rsrc(A);
    const unsigned lds0 = (unsigned)(size_t)(LAS char*)smem;
    u64 tag;
    CvIn cin;

    KgVals kv;
    int hflag;
    kg_issue(dp, A, lds0);
    if (ix < 28) {
        const int rb = (xcd & 3) * 4 + (ix & 3), cb = (xcd >> 2) * 7 + (ix >> 2);
        const int wm = wave & 1, wn = wave >> 1;
        unsigned xoff[2], woff[2];
#pragma unroll
        for (int i = 0; i < 2; ++i) {
            const int ci = (wave * 2 + i) * 64 + lane, row = ci >> 3, pos = ci & 7, ch = pos ^ ((row >> 1) & 7);
            xoff[i] = ((rb * 128 + row) * DM + ch * 8) * 2 - i * 1024 + 4096;
        }
#pragma unroll
        for (int i = 0; i < 2; ++i) {
            const int ci = ((wave & 3) * 2 + i) * 64 + lane, k = ci >> 4, pos = ci & 15;
            const int f = (k & 3) | (((k >> 3) & 1) << 2);
            const int c = (((pos >> 1) ^ f) << 1) | (pos & 1);
            woff[i] = (k * FF + cb * 128 + c * 8) * 2 - i * 1024 + 4096;
        }
        const char* xb = (const char*)A.x - 4096;
        const char* wb = (const char*)((wave >> 2) ? A.wu : A.wg) - 4096;
        const unsigned xdst = lds0 + wave * 2048, wdst = lds0 + NXS * XS + wave * 2048;
        auto m0x = [&](int X) { set_m0(xdst + (X % NXS) * XS); };
        auto m0w = [&](int j) { set_m0(wdst + (j % NWS) * WSL); };
#define LDX(X, I) glds_o<I>(xb + (X) * 128, xoff[I])
#define LDW(J, I) glds_o<I>(wb + (size_t)(J) * (32 * FF * 2), woff[I])
#define LDX2(X) do { m0x(X); asm volatile("s_nop 0"); LDX(X, 0); LDX(X, 1); } while (0)
#define LDW2(J) do { m0w(J); asm volatile("s_nop 0"); LDW(J, 0); LDW(J, 1); } while (0)
        LDX2(0); LDW2(0);
        SB();
        const int m = lane & 15, g = lane >> 4, q = (lane & 15) >> 2, p = lane & 3;
        const int fx = (m >> 1) & 7;
        int xrd[2];
#pragma unroll
        for (int ks = 0; ks < 2; ++ks) xrd[ks] = lds0 + (wm * 64 + m) * 128 + (((4 * ks + g) ^ fx) << 4);
        const int fw = q | ((g & 1) << 2);
        int wrd[2][4];
#pragma unroll
        for (int f = 0; f < 4; ++f) {
            wrd[0][f] = lds0 + NXS * XS + (8 * g + q) * 256 + (((wn * 2 + (f & 1)) ^ fw) << 5) + p * 8 + ((f >> 1) ^ wm) * 8192;
            wrd[1][f] = wrd[0][f] + 3 * WSL;
        }
        const char* pfa = nullptr;
        if (!PF) {} else if (tid < 352) { const int idx = (ix & 3) * 352 + tid, mat = idx >= 704, rem = idx - 704 * mat;
            pfa = (const char*)(mat ? A.wu : A.wg) + (size_t)(160 + (rem >> 1)) * (FF * 2) + cb * 256 + (rem & 1) * 128; }
        else { const int idx = min((ix >> 2) * 110 + min(tid - 352, 109), 767), row = idx / 6;
            pfa = (const char*)A.x + (size_t)(rb * 128 + row) * 1024 + (2 + idx - 6 * row) * 128; }
        wait_vm<4>();
        {
            const KgIn kin = kg_read(smem);
            ST(1);
            kgen_values(kin, kv, [&](int k) {
                SB();
                if (k == 0) LDW2(1); else if (k == 1) LDX2(1); else if (k == 2) LDW2(2); else if (k == 3) LDW2(3); else LDW2(4);
                SB();
            });
            if (wave == 0) epoch_issue(A, lds0);
        }
        f32x4 acc[4][4];
#pragma unroll
        for (int i = 0; i < 4; ++i)
#pragma unroll
            for (int j = 0; j < 4; ++j) acc[i][j] = f32x4{0, 0, 0, 0};
        f32x4 csa[2] = {f32x4{0, 0, 0, 0}, f32x4{0, 0, 0, 0}};
        const s16x8 ones = {0x3f80, 0x3f80, 0x3f80, 0x3f80, 0x3f80, 0x3f80, 0x3f80, 0x3f80};
        s16x8 xf[2][4];
        s16x4 wlo[4], whi[4];
        auto rdx = [&](int buf, int j, int mt) { xf[buf][mt] = *(const LAS s16x8*)(LP(xrd[j & 1]) + ((j >> 1) % NXS) * XS + mt * 2048); };
        auto rdwl = [&](int j, int f) {
            const int slot = j % NWS;
            wlo[f] = __builtin_amdgcn_ds_read_tr16_b64_v4i16((LAS s16x4*)(LP(wrd[slot >= 3][f]) + (slot % 3) * WSL));
        };
        auto rdwh = [&](int j, int f) {
            const int slot = j % NWS;
            whi[f] = __builtin_amdgcn_ds_read_tr16_b64_v4i16((LAS s16x4*)(LP(wrd[slot >= 3][f]) + (slot % 3) * WSL + 1024));
        };
        if (wave == 0) wait_vm<11>(); else wait_vm<10>();
        BAR();
        ST(2);
        if (PF) touch(lds0, pfa);
#pragma unroll
        for (int mt = 0; mt < 4; ++mt) rdx(0, 0, mt);
#pragma unroll
        for (int f = 0; f < 4; ++f) { rdwl(0, f); rdwh(0, f); }
        SB();
        const u64* pl = fl + FL_LN + (lane & 31);
#pragma unroll
        for (int j = 0; j < 16; ++j) {
            if (j < 15) {
                auto iss = [](int w) { return w < 0 ? 0 : ((w + 5 < 16 ? 2 : 0) + ((w & 1) == 0 && w / 2 + 2 < 8 ? 2 : 0)); };
                int allow;
                if (j == 0) allow = 8 + PF;
                else if (j == 1) allow = 4 + PF + iss(0);
                else if (j == 2) allow = 2 + PF + iss(0) + iss(1);
                else if (j == 3) allow = iss(1) + iss(2);
                else allow = (j & 1) ? iss(j - 2) + iss(j - 1) : iss(j - 3) + iss(j - 2) + iss(j - 1);
                if (j == 14 && wave == 0) wait_vm<1>();
                else if (j <= 2 && wave == 0) {
                    if (allow == 8) wait_vm<9>(); else if (allow == 9) wait_vm<10>(); else wait_vm<0>();
                }
                else switch (allow) {
#define WV(n) case n: wait_vm<n>(); break;
                    WV(0) WV(1) WV(2) WV(3) WV(4) WV(5) WV(6) WV(7) WV(8) WV(9) WV(10) WV(11) WV(12)
#undef WV
                    default: wait_vm<0>(); break;
                }
                BAR();
                SB();
            }
            if (j == 4) { tag = epoch_read(smem); SB(); }
            if (j == 13) { if (wave == 0) poll_issue(pl); SB(); }
            const int cur = j & 1, nxt = cur ^ 1;
            const bool rd = j < 15, lw = j + 5 < 16, lx = (j & 1) == 0 && j / 2 + 2 < 8;
#pragma unroll
            for (int f = 0; f < 4; ++f) {
                const s16x8 wf = __builtin_shufflevector(wlo[f], whi[f], 0, 1, 2, 3, 4, 5, 6, 7);
                acc[f][0] = mfma16(wf, xf[cur][0], acc[f][0]);
                if (rd) rdx(nxt, j + 1, f);
                SB();
                acc[f][1] = mfma16(wf, xf[cur][1], acc[f][1]);
                if (f == 0 && lx) m0x(j / 2 + 2);
                if (f == 1 && lx) LDX(j / 2 + 2, 0);
                if (f == 2 && lw) m0w(j + 5);
                if (f == 3 && lw) LDW(j + 5, 0);
                if (f < 2) csa[f] = mfma16(wf, ones, csa[f]);
                SB();
                acc[f][2] = mfma16(wf, xf[cur][2], acc[f][2]);
                if (rd) rdwl(j + 1, f);
                SB();
                acc[f][3] = mfma16(wf, xf[cur][3], acc[f][3]);
                if (rd) rdwh(j + 1, f);
                if (f == 1 && lx) LDX(j / 2 + 2, 1);
                if (f == 3 && lw) LDW(j + 5, 1);
                SB();
            }
        }
#undef LDX
#undef LDW
#undef LDX2
#undef LDW2
        ST(3);
        if (wave == 0) poll_check<8>(pl, tag DBG_PASS(1));
        if (tid < 8) *(unsigned*)(smem + CTL_OFF + tid * 4) = 0;
        if (m == 0) { f32x4* c = (f32x4*)(smem + CS_OFF) + (wn * 4 + 2 * wm) * 4 + g; c[0] = csa[0]; c[4] = csa[1]; }
        LBAR();
        u32x2 stt[4];
        f32x4 csv[4];
#pragma unroll
        for (int mt = 0; mt < 4; ++mt) stt[mt] = ld_pub8(rs, WS_STATS + (rb * 128 + wm * 64 + 16 * mt + m) * 8);
#pragma unroll
        for (int f = 0; f < 4; ++f) csv[f] = *(const f32x4*)(smem + CS_OFF + ((wn * 4 + f) * 16 + 4 * g) * 4);
        if (wave < 4) {
            cin.u[0] = ld_pub16(rs, WS_XNP + (dp * SL + tid * 4) * 4);
            cin.u[1] = ld_pub16(rs, WS_XNP + (dp * SL + 1024 + tid * 4) * 4);
            cin.ds = A.dskip[dp * 2 + (tid >> 7)];
        }
        SB();
        kgen_tables(kv, smem);
        ST(4);
        const int kt = cb * 4 + wn;
        auto epi = [&](auto GS) __attribute__((always_inline)) {
            constexpr int gs = decltype(GS)::value, us = 2 - gs;
#pragma unroll
            for (int mt = 0; mt < 4; ++mt) {
                const float mb = __uint_as_float(stt[mt][0]), rd = __uint_as_float(stt[mt][1]);
                const float nl = rd * -1.44269504088896341f, ird2 = __builtin_amdgcn_rcpf(rd * rd);
                u32x4 o;
#pragma unroll
                for (int u = 0; u < 2; ++u)
#pragma unroll
                    for (int r = 0; r < 2; ++r) {
                        const f32x2 gp = f32x2{acc[gs + u][mt][2 * r], acc[gs + u][mt][2 * r + 1]} - mb * f32x2{csv[u][2 * r], csv[u][2 * r + 1]};
                        const f32x2 up = f32x2{acc[us + u][mt][2 * r], acc[us + u][mt][2 * r + 1]} - mb * f32x2{csv[2 + u][2 * r], csv[2 + u][2 * r + 1]};
                        const f32x2 t = gp * nl;
                        const f32x2 e = {__builtin_amdgcn_exp2f(t[0]), __builtin_amdgcn_exp2f(t[1])};
                        const f32x2 d = __builtin_elementwise_fma(e, f32x2{ird2, ird2}, f32x2{ird2, ird2});
                        const f32x2 h = gp * up * f32x2{__builtin_amdgcn_rcpf(d[0]), __builtin_amdgcn_rcpf(d[1])};
                        o[2 * u + r] = pk2(h[0], h[1]);
                    }
                st_pub16(rs, hf_tile(rb * 8 + wm * 4 + mt) + kt * 1024 + lane * 16, o);
            }
        };
        if (wm) epi(std::integral_constant<int, 2>{}); else epi(std::integral_constant<int, 0>{});
        if (tid == 0) st_flag(fl + FL_ARR + b, tag);
        hflag = FL_H + rb * 16 + cb;
    } else {
        const int a = (ix - 28) * 8 + xcd;
        AuxX ax;
        aux_load_x(a, A, ax);
        if (wave == 0) epoch_issue(A, lds0);
        if (wave < 4) cin.ds = A.dskip[dp * 2 + (tid >> 7)];
        wait_vm<0>();
        const KgIn kin = kg_read(smem);
        if (tid < 8) *(unsigned*)(smem + CTL_OFF + tid * 4) = 0;
        LBAR();
        tag = epoch_read(smem);
        if (tid == 0) st_flag(fl + FL_ARR + b, tag);
        ln_part(a, A, ax, smem);
        ST(1);
        if (a < 28) {
            AuxD ad;
            SB();
            aux_load_d(a, A, ad);
            SB();
            wait_vm<8>();
            LBAR();
            if (tid == 0) st_flag(fl + FL_LN + a, tag);
            ST(2);
            wd_part(a, A, ad, smem);
            wait_vm<0>();
            LBAR();
            if (tid == 0) st_flag(fl + FL_WD + a, tag);
        } else {
            wait_vm<0>();
            LBAR();
            if (tid == 0) st_flag(fl + FL_LN + a, tag);
        }
        ST(3);
        kgen_values(kin, kv, [](int) {});
        kgen_tables(kv, smem);
        ST(4);
        if (wave == 0) wait_flags<8>(fl + FL_LN + (lane & 31), tag DBG_PASS(2));
        LBAR();
        if (wave < 4) {
            cin.u[0] = ld_pub16(rs, WS_XNP + (dp * SL + tid * 4) * 4);
            cin.u[1] = ld_pub16(rs, WS_XNP + (dp * SL + 1024 + tid * 4) * 4);
        }
        hflag = -1;
    }
    if (wave == 0) poll_issue(fl + FL_WD + min(lane, 27));
    LBAR();
    ST(5);
    kgen_contract(wave >> 2, wave & 3, smem);
    if (wave == 0) poll_check<8>(fl + FL_WD + min(lane, 27), tag DBG_PASS(6));
    wait_vm<0>();
    LBAR();
    if (hflag >= 0 && tid == 0) st_flag(fl + hflag, tag);
    ST(6);
    const int ft = tid >> 3, fc = tid & 7;
    const u32x4 xv = *(const u32x4*)(A.x + (size_t)(rb2 * 64 + ft) * DM + cb2 * 64 + fc * 8);
    if (wave >= 4) {
        const int kw = wave - 4;
        const unsigned hb = hf_tile(rb2 * 4) + kw * 14 * 1024 + lane * 16;
        const unsigned wb = WS_WDF + ((cb2 * 4) * 56 + kw * 14) * 1024 + lane * 16;
        team_wait_flags<2>(wave == 4, lds0 + CTL_OFF + 24, fl + FL_H + (rb2 >> 1) * 16 + min(lane & 15, 13), tag DBG_PASS(3));
        ST(8);
        f32x4 acc[4][4];
#pragma unroll
        for (int i = 0; i < 4; ++i)
#pragma unroll
            for (int j = 0; j < 4; ++j) acc[i][j] = f32x4{0, 0, 0, 0};
        constexpr int PD = G2PD;
        s16x8 Af[PD][4], Bf[PD][4];
        auto lda = [&](int kt, int i) { Af[kt % PD][i] = __builtin_bit_cast(s16x8, ld_pub16(rs, wb + kt * 1024, i * 57344)); };
        auto ldb = [&](int kt, int i) { Bf[kt % PD][i] = __builtin_bit_cast(s16x8, ld_pub16(rs, hb + kt * 1024, i * 57344)); };
#pragma unroll
        for (int kt = 0; kt < PD - 1; ++kt) {
#pragma unroll
            for (int i = 0; i < 4; ++i) { lda(kt, i); ldb(kt, i); }
            SB();
        }
#pragma unroll
        for (int kt = 0; kt < 14; ++kt) {
            if (kt == G2SIG && lane == 0) lds_add(lds0 + CTL_OFF + 20);
            const bool ld = kt + PD - 1 < 14;
#pragma unroll
            for (int i = 0; i < 4; ++i) {
#pragma unroll
                for (int j = 0; j < 4; ++j) {
                    acc[i][j] = mfma16(Af[kt % PD][i], Bf[kt % PD][j], acc[i][j]);
                    if (ld && j == 0) lda(kt + PD - 1, i);
                    if (ld && j == 2) ldb(kt + PD - 1, i);
                    SB();
                }
            }
        }
        ST(10);
        const int m = lane & 15, g = lane >> 4;
#pragma unroll
        for (int i = 0; i < 4; ++i)
#pragma unroll
            for (int j = 0; j < 4; ++j) *(f32x4*)(smem + G2_P + ((kw * 4 + i) * 4 + j) * 1024 + (16 * g + (m ^ (4 * i + g))) * 16) = acc[i][j];
    } else {
        {
            KIdx kix;
            conv_kindex(kix);
            conv_uimage(cin, smem);
            conv_kimage(0, kix, smem);
            conv_kimage(1, kix, smem);
        }
        team_bar(lds0 + CTL_OFF + 16, 4);
        ST(7);
        f32x16 acc0, acc1;
        conv_mma(wave, smem, acc0, acc1);
        ST(8);
        conv_finish(wave, dp, A, cin.ds, smem, acc0, acc1);
        ST(9);
        wait_vm<0>();
        if (lane == 0 && lds_add(lds0 + CTL_OFF) == 3) st_flag(fl + FL_S + dp, tag);
        if (wave == 0) while (__builtin_amdgcn_readfirstlane(lds_ld(lds0 + CTL_OFF + 20)) < 4) __builtin_amdgcn_s_sleep(2);
        team_wait_flags<4>(wave == 0, lds0 + CTL_OFF + 28, fl + FL_S + cb2 * 32 + (lane & 31), tag DBG_PASS(4));
#pragma unroll
        for (int i = 0; i < 2; ++i) {
            const int d = (tid >> 3) + 32 * i;
            const u32x4 sv = ld_pub16(rs, WS_SP + ((cb2 * 64 + d) * SL + rb2 * 64 + (tid & 7) * 8) * 2);
            *(u32x4*)(smem + G2_S + d * 128 + (((tid & 7) ^ ((d >> 3) & 7)) << 4)) = sv;
        }
        ST(10);
    }
    LBAR();
    ST(11);
    {
        const int mt = ft >> 4, m = ft & 15, dt = fc >> 1, g0 = 2 * (fc & 1);
        f32x4 y0 = {0, 0, 0, 0}, y1 = {0, 0, 0, 0};
#pragma unroll
        for (int s = 0; s < 4; ++s) {
            const char* base = smem + G2_P + ((s * 4 + dt) * 4 + mt) * 1024;
            y0 += *(const f32x4*)(base + (16 * g0 + (m ^ (4 * dt + g0))) * 16);
            y1 += *(const f32x4*)(base + (16 * (g0 + 1) + (m ^ (4 * dt + g0 + 1))) * 16);
        }
        const u16* S = (const u16*)(smem + G2_S + (8 * fc) * 128 + (((ft >> 3) ^ fc) << 4)) + (ft & 7);
        unsigned o[4];
#pragma unroll
        for (int j = 0; j < 4; ++j) {
            const f32x2 f = {rbf(j < 2 ? y0[2 * j] : y1[2 * j - 4]), rbf(j < 2 ? y0[2 * j + 1] : y1[2 * j - 3])};
            const f32x2 sv = {bf2f(S[(2 * j) * 64]), bf2f(S[(2 * j + 1) * 64])};
            const f32x2 gg = {rbf(f[0] * sigm(f[0])), rbf(f[1] * sigm(f[1]))};
            const f32x2 sg = sv * gg;
            o[j] = pk2(bflo(xv[j]) + rbf(sg[0]), bfhi(xv[j]) + rbf(sg[1]));
        }
        *(u32x4*)(A.out + (size_t)(rb2 * 64 + ft) * DM + cb2 * 64 + fc * 8) = u32x4{o[0], o[1], o[2], o[3]};
    }
    ST(12);
    if (b == 0) {
        if (wave < 4) wait_flags(fl + FL_ARR + wave * 64 + lane, tag DBG_PASS(5));
        LBAR();
        if (tid == 0) st_flag(fl + FL_EPOCH, tag);
    }
    ST(15); ST_FLUSH(A.ws, (int)(tag & 1));
}

extern "C" void kernel_launch(void* const* d_in, const int* in_sizes, int n_in, void* d_out, int out_size, void* d_ws, size_t ws_size,
                              hipStream_t stream) {
    Args A;
    A.x = (const u16*)d_in[0]; A.Are = (const u16*)d_in[1]; A.Aim = (const u16*)d_in[2]; A.Bre = (const u16*)d_in[3];
    A.Bim = (const u16*)d_in[4]; A.Cre = (const u16*)d_in[5]; A.Cim = (const u16*)d_in[6]; A.logdt = (const u16*)d_in[7];
    A.dskip = (const u16*)d_in[8]; A.wg = (const u16*)d_in[10]; A.wu = (const u16*)d_in[11]; A.wd = (const u16*)d_in[12];
    A.out = (u16*)d_out; A.ws = (char*)d_ws;
    (void)hipFuncSetAttribute((const void*)k_fused, hipFuncAttributeMaxDynamicSharedMemorySize, FUSED_LDS);
    k_fused<<<256, 512, FUSED_LDS, stream>>>(A);
}
```
